```python
import torch

def _softmax(x, axis=-1):
    return torch.softmax(x, dim=axis)
def _gelu(x):
    return torch.nn.functional.gelu(x, approximate="tanh")
def _silu(x):
    return torch.nn.functional.silu(x)
def _sigmoid(x):
    return torch.sigmoid(x)
def _layernorm(x, axis=-1, eps=1e-5):
    m = x.mean(dim=axis, keepdim=True)
    v = x.var(dim=axis, keepdim=True, unbiased=False)
    return (x - m) / torch.sqrt(v + eps)
def _scatter_add(tgt, idx, src, axis=0):
    return tgt.clone().index_add_(axis, idx, src)
def _segment_sum(src, seg, n):
    out = torch.zeros((n,) + src.shape[1:], dtype=src.dtype)
    return out.index_add_(0, seg, src)
def _pad(x, pw):
    flat = tuple(v for pair in reversed(pw) for v in pair)
    return torch.nn.functional.pad(x, flat)

kernel_name = 'recurrent_scan__ssm_conv_s5__dense_activation_gelu__prompt'

def setup_inputs():

    _w = (lambda a: (lambda t: t.to(torch.bfloat16) if t.is_floating_point() else t)(torch.as_tensor(a)))
    def _A_setup():
        B, S, D = 1, 2048, 640
        P = D
        Lambda_re = -0.5 * (lambda _x: torch.exp(torch.as_tensor(_x)))((lambda *s: torch.randn(*s, dtype=torch.bfloat16) * 0.4)(P))
        Lambda_im = (lambda *s: torch.randn(*s, dtype=torch.bfloat16) * 0.8)(P) * 1.0
        B_re = (lambda *s: torch.randn(*s, dtype=torch.bfloat16) * 0.8)(P, D) * (D ** -0.5)
        B_im = (lambda *s: torch.randn(*s, dtype=torch.bfloat16) * 0.8)(P, D) * (D ** -0.5)
        C_re = (lambda *s: torch.randn(*s, dtype=torch.bfloat16) * 0.4)(D, P) * (P ** -0.5)
        C_im = (lambda *s: torch.randn(*s, dtype=torch.bfloat16) * 0.8)(D, P) * (P ** -0.5)
        log_dt = (lambda *s: torch.randn(*s, dtype=torch.bfloat16) * 0.4)(P) * 0.3 - 2.0
        Dskip = (lambda *s: torch.randn(*s, dtype=torch.bfloat16) * 0.2)(D)
        return {
            "x": _w((lambda *s: torch.randn(*s, dtype=torch.bfloat16) * 0.05)(B, S, D)),
            "Lambda_re": _w(Lambda_re),
            "Lambda_im": _w(Lambda_im),
            "B_re": _w(B_re),
            "B_im": _w(B_im),
            "C_re": _w(C_re),
            "C_im": _w(C_im),
            "log_dt": _w(log_dt),
            "Dskip": _w(Dskip),
        }
    _A_in = _A_setup()

    def _B_setup():
        B, S, D, F = 1, 2048, 640, 1536
        return {
            "x": _w((lambda *s: torch.randn(*s, dtype=torch.bfloat16) * 0.05)(B, S, D)),
            "w1": _w((lambda *s: torch.randn(*s, dtype=torch.bfloat16) * 0.8)(D, F) * (D ** -0.5)),
            "b1": _w((lambda *s: torch.randn(*s, dtype=torch.bfloat16) * 12.8)(F) * 0.01),
            "w2": _w((lambda *s: torch.randn(*s, dtype=torch.bfloat16) * 1.6)(F, D) * (F ** -0.5)),
            "b2": _w((lambda *s: torch.randn(*s, dtype=torch.bfloat16) * 12.8)(D) * 0.01),
        }
    _B_in = _B_setup()

    return {
        "A_x": _A_in["x"],
        "A_Lambda_re": _A_in["Lambda_re"],
        "A_Lambda_im": _A_in["Lambda_im"],
        "A_B_re": _A_in["B_re"],
        "A_B_im": _A_in["B_im"],
        "A_C_re": _A_in["C_re"],
        "A_C_im": _A_in["C_im"],
        "A_log_dt": _A_in["log_dt"],
        "A_Dskip": _A_in["Dskip"],
        "B_x": _B_in["x"],
        "B_w1": _B_in["w1"],
        "B_b1": _B_in["b1"],
        "B_w2": _B_in["w2"],
        "B_b2": _B_in["b2"],
    }

def reference(A_x, A_Lambda_re, A_Lambda_im, A_B_re, A_B_im, A_C_re, A_C_im, A_log_dt, A_Dskip, B_x, B_w1, B_b1, B_w2, B_b2):
    def _A_ref(x, Lambda_re, Lambda_im, B_re, B_im, C_re, C_im, log_dt, Dskip):
        Bsz, L, H = x.shape
        Lam = Lambda_re + 1j * Lambda_im
        Bmat = B_re + 1j * B_im
        Cmat = C_re + 1j * C_im
        dt = torch.exp(log_dt)
        Lam_bar = torch.exp(dt * Lam)
        B_bar = ((Lam_bar - 1.0) / Lam)[:, None] * Bmat
        Bu = torch.einsum("bld,pd->blp", x + 0j, B_bar)
        h = torch.zeros_like(Bu[:, 0, :])
        hs = []
        for t in range(L):
            h = Lam_bar[None, :] * h + Bu[:, t, :]
            hs.append(h)
        hseq = torch.stack(hs, 1)
        y = 2.0 * torch.einsum("blp,dp->bld", hseq, Cmat).real + x * Dskip[None, None, :]
        return y

    def _B_ref(x, w1, b1, w2, b2):
        h = torch.einsum("bsd,df->bsf", x, w1) + b1
        h = _gelu(h)
        return torch.einsum("bsf,fd->bsd", h, w2) + b2

    x = A_x
    xn = _layernorm(x, axis=-1)
    s = _A_ref(xn, A_Lambda_re, A_Lambda_im, A_B_re, A_B_im, A_C_re, A_C_im, A_log_dt, A_Dskip)
    g = _silu(_B_ref(xn, B_w1, B_b1, B_w2, B_b2))
    return x + s * g
```

```cpp
#include <hip/hip_runtime.h>
#include <hip/hip_bf16.h>
#include <stdint.h>

namespace {
constexpr int L = 2048, D = 640, P = 640, F = 1536;
typedef unsigned short u16;
__device__ __forceinline__ float bf2f(u16 b) { return __builtin_bit_cast(float, (unsigned)b << 16); }
__device__ __forceinline__ u16 f2bf(float f) { unsigned u = __builtin_bit_cast(unsigned, f); u += 0x7FFFu + ((u >> 16) & 1u); return (u16)(u >> 16); }

__global__ void k_ln(const u16* x, float* xn) {
  int row = blockIdx.x; int t = threadIdx.x;
  float v[10]; float s = 0;
  for (int i = 0; i < 10; ++i) { v[i] = bf2f(x[row * D + t + 64 * i]); s += v[i]; }
  for (int o = 32; o; o >>= 1) s += __shfl_xor(s, o);
  float m = s / D; float q = 0;
  for (int i = 0; i < 10; ++i) { float d = v[i] - m; q += d * d; }
  for (int o = 32; o; o >>= 1) q += __shfl_xor(q, o);
  float r = rsqrtf(q / D + 1e-5f);
  for (int i = 0; i < 10; ++i) xn[row * D + t + 64 * i] = (v[i] - m) * r;
}
__global__ void k_bu(const float* xn, const u16* Bre, const u16* Bim, float* bur, float* bui) {
  int p = blockIdx.x * 64 + threadIdx.x; int row = blockIdx.y;
  float a = 0, b = 0;
  for (int d = 0; d < D; ++d) { float xv = xn[row * D + d]; a += xv * bf2f(Bre[p * D + d]); b += xv * bf2f(Bim[p * D + d]); }
  bur[row * P + p] = a; bui[row * P + p] = b;
}
__global__ void k_scan(const u16* lre, const u16* lim, const u16* logdt, float* bur, float* bui) {
  int p = blockIdx.x * 64 + threadIdx.x;
  float dt = bf2f(f2bf(expf(bf2f(logdt[p]))));
  float lr = bf2f(lre[p]), li = bf2f(lim[p]);
  float mag = expf(dt * lr); float ar = mag * cosf(dt * li), ai = mag * sinf(dt * li);
  float nr = ar - 1.f, ni = ai; float den = lr * lr + li * li;
  float cr = (nr * lr + ni * li) / den, ci = (ni * lr - nr * li) / den;
  float hr = 0, hi = 0;
  for (int t = 0; t < L; ++t) {
    float ur = bur[t * P + p], ui = bui[t * P + p];
    float br = cr * ur - ci * ui, bi = cr * ui + ci * ur;
    float nhr = ar * hr - ai * hi + br, nhi = ar * hi + ai * hr + bi;
    hr = nhr; hi = nhi;
    bur[t * P + p] = hr; bui[t * P + p] = hi;
  }
}
__global__ void k_ffn1(const float* xn, const u16* w1, const u16* b1, float* h1) {
  int f = blockIdx.x * 64 + threadIdx.x; int row = blockIdx.y;
  float a = 0;
  for (int d = 0; d < D; ++d) a += xn[row * D + d] * bf2f(w1[d * F + f]);
  a += bf2f(b1[f]);
  float u = 0.7978845608028654f * (a + 0.044715f * a * a * a);
  h1[row * F + f] = 0.5f * a * (1.f + tanhf(u));
}
__global__ void k_out(const u16* x, const float* xn, const float* hr, const float* hi, const u16* Cre, const u16* Cim, const u16* Dsk,
                      const float* h1, const u16* w2, const u16* b2, float* out) {
  int d = blockIdx.x * 64 + threadIdx.x; int row = blockIdx.y;
  float y = 0;
  for (int p = 0; p < P; ++p) y += hr[row * P + p] * bf2f(Cre[d * P + p]) - hi[row * P + p] * bf2f(Cim[d * P + p]);
  y = 2.f * y + xn[row * D + d] * bf2f(Dsk[d]);
  float g = 0;
  for (int f = 0; f < F; ++f) g += h1[row * F + f] * bf2f(w2[f * D + d]);
  g += bf2f(b2[d]);
  g = g / (1.f + expf(-g));
  out[row * D + d] = bf2f(x[row * D + d]) + y * g;
}
}

extern "C" void kernel_launch(void* const* d_in, const int* in_sizes, int n_in,
                              void* d_out, int out_size, void* d_ws, size_t ws_size,
                              hipStream_t stream) {
  const u16* x = (const u16*)d_in[0];
  const u16 *lre = (const u16*)d_in[1], *lim = (const u16*)d_in[2];
  const u16 *Bre = (const u16*)d_in[3], *Bim = (const u16*)d_in[4], *Cre = (const u16*)d_in[5], *Cim = (const u16*)d_in[6];
  const u16 *logdt = (const u16*)d_in[7], *Dsk = (const u16*)d_in[8];
  const u16 *w1 = (const u16*)d_in[10], *b1 = (const u16*)d_in[11], *w2 = (const u16*)d_in[12], *b2 = (const u16*)d_in[13];
  float* ws = (float*)d_ws;
  float* xn = ws; float* bur = xn + L * D; float* bui = bur + L * P; float* h1 = bui + L * P;
  k_ln<<<L, 64, 0, stream>>>(x, xn);
  k_bu<<<dim3(P / 64, L), 64, 0, stream>>>(xn, Bre, Bim, bur, bui);
  k_scan<<<P / 64, 64, 0, stream>>>(lre, lim, logdt, bur, bui);
  k_ffn1<<<dim3(F / 64, L), 64, 0, stream>>>(xn, w1, b1, h1);
  k_out<<<dim3(D / 64, L), 64, 0, stream>>>(x, xn, bur, bui, Cre, Cim, Dsk, h1, w2, b2, (float*)d_out);
}
```

```cpp
#include <hip/hip_runtime.h>
#include <hip/hip_bf16.h>
#include <stdint.h>

namespace s5k {
typedef unsigned short u16;
typedef unsigned long long u64;
typedef unsigned v4u __attribute__((ext_vector_type(4)));
typedef unsigned v2u __attribute__((ext_vector_type(2)));
typedef float f32x4 __attribute__((ext_vector_type(4)));
typedef float f32x2 __attribute__((ext_vector_type(2)));
typedef float f32x16 __attribute__((ext_vector_type(16)));
typedef __bf16 bf16x8 __attribute__((ext_vector_type(8)));
typedef __bf16 bf16x2 __attribute__((ext_vector_type(2)));
typedef short s16x4 __attribute__((ext_vector_type(4)));
#define GAS __attribute__((address_space(1)))
#define LAS __attribute__((address_space(3)))
#define RLX_AGENT __ATOMIC_RELAXED, __HIP_MEMORY_SCOPE_AGENT
#define RLX_WG __ATOMIC_RELAXED, __HIP_MEMORY_SCOPE_WORKGROUP

constexpr int L = 2048, D = 640, P = 640, F = 1536;
constexpr int NWG = 256, NTHR = 512;
constexpr int EPOCH_OFF = 0;
constexpr int FLAG_W_OFF = 4096;
constexpr int FLAG_F_OFF = 65536;
constexpr int FLAG_H_OFF = 65536 + 8192;
constexpr int EG_OFF = 131072;
constexpr int WT_OFF = 1 << 20;
constexpr int H1T_OFF = 8 << 20;
constexpr int HT_OFF = 16 << 20;
constexpr int WS_BYTES = 24 << 20;
constexpr int XS = 0, BWS = 16384, W1S = 26624, STAGE = 38912, NSTAGE = 4;
constexpr int MISC = NSTAGE * STAGE;
constexpr int ST_RS = MISC, ST_MB = MISC + 512;
constexpr int CSB = MISC + 1024;
constexpr int CSF = MISC + 2304;
constexpr int CSW = MISC + 3840;
constexpr int B1F = MISC + 5376;
constexpr int PRM = MISC + 5760;
constexpr int CNT = MISC + 7040;
constexpr int LSTAMP = MISC + 7168;
constexpr int LDS_BYTES = MISC + 8192;
constexpr int YREG = 0;
constexpr int FREG = 81920;
constexpr int ESR = FREG + 4 * 15360;
static_assert(ESR + 5120 <= MISC, "");
static_assert(LDS_BYTES <= 163840, "");
constexpr unsigned SPIN_CAP = 1u << 20;
constexpr int AUX_SC1 = 16;

__device__ __forceinline__ float bf2f(unsigned b) { return __builtin_bit_cast(float, b << 16); }
__device__ __forceinline__ float bfhi(unsigned b) { return __builtin_bit_cast(float, b & 0xffff0000u); }
__device__ __forceinline__ unsigned pk2(float a, float b) { f32x2 f = {a, b}; return __builtin_bit_cast(unsigned, __builtin_convertvector(f, bf16x2)); }
__device__ __forceinline__ float rbf(float a) { return bf2f(pk2(a, a) & 0xffffu); }
__device__ __forceinline__ float dot2(unsigned a, unsigned b, float c) {
  return __builtin_amdgcn_fdot2_f32_bf16(__builtin_bit_cast(bf16x2, a), __builtin_bit_cast(bf16x2, b), c, false);
}
__device__ __forceinline__ float sum8(v4u a, float c) {
  const unsigned ones = 0x3f803f80u;
  c = dot2(a.x, ones, c); c = dot2(a.y, ones, c); c = dot2(a.z, ones, c); c = dot2(a.w, ones, c); return c;
}
__device__ __forceinline__ float swp1(float v) {
  return __builtin_bit_cast(float, __builtin_amdgcn_update_dpp(0, __builtin_bit_cast(int, v), 0xB1, 0xF, 0xF, true));
}
__device__ __forceinline__ f32x4 mfma16(v4u a, v4u b, f32x4 c) {
  return __builtin_amdgcn_mfma_f32_16x16x32_bf16(__builtin_bit_cast(bf16x8, a), __builtin_bit_cast(bf16x8, b), c, 0, 0, 0);
}
__device__ __forceinline__ f32x16 mfma32(v4u a, v4u b, f32x16 c) {
  return __builtin_amdgcn_mfma_f32_32x32x16_bf16(__builtin_bit_cast(bf16x8, a), __builtin_bit_cast(bf16x8, b), c, 0, 0, 0);
}
__device__ __forceinline__ v2u trread(LAS unsigned char* p) {
  return __builtin_bit_cast(v2u, __builtin_amdgcn_ds_read_tr16_b64_v4i16((LAS s16x4*)p));
}
__device__ __forceinline__ void stage5(const void* xb, unsigned xo0, unsigned xo1, unsigned xd, const void* wb, unsigned wo0, unsigned wo1, unsigned wo2, unsigned wd) {
  unsigned keep;
  asm volatile("s_mov_b32 %0, m0\n\t"
               "s_mov_b32 m0, %4\n\ts_nop 0\n\tglobal_load_lds_dwordx4 %2, %1\n\t"
               "s_add_u32 m0, m0, 0x400\n\ts_nop 0\n\tglobal_load_lds_dwordx4 %3, %1\n\t"
               "s_mov_b32 m0, %9\n\ts_nop 0\n\tglobal_load_lds_dwordx4 %6, %5\n\t"
               "s_add_u32 m0, m0, 0x400\n\ts_nop 0\n\tglobal_load_lds_dwordx4 %7, %5\n\t"
               "s_add_u32 m0, m0, 0x400\n\ts_nop 0\n\tglobal_load_lds_dwordx4 %8, %5\n\t"
               "s_mov_b32 m0, %0"
               : "=&s"(keep) : "s"(xb), "v"(xo0), "v"(xo1), "s"(xd), "s"(wb), "v"(wo0), "v"(wo1), "v"(wo2), "s"(wd) : "memory", "scc");
}
__device__ __forceinline__ void stage5h(const void* xb, unsigned xo0, unsigned xo1, unsigned xd, const void* wb, unsigned wo0, unsigned wo1, unsigned wo2, unsigned wd) {
  unsigned keep; u64 ex;
  asm volatile("s_mov_b32 %0, m0\n\t"
               "s_mov_b32 m0, %5\n\ts_nop 0\n\tglobal_load_lds_dwordx4 %3, %2\n\t"
               "s_add_u32 m0, m0, 0x400\n\ts_nop 0\n\tglobal_load_lds_dwordx4 %4, %2\n\t"
               "s_mov_b32 m0, %10\n\ts_nop 0\n\tglobal_load_lds_dwordx4 %7, %6\n\t"
               "s_add_u32 m0, m0, 0x400\n\ts_nop 0\n\tglobal_load_lds_dwordx4 %8, %6\n\t"
               "s_add_u32 m0, m0, 0x400\n\ts_mov_b64 %1, exec\n\ts_mov_b32 exec_hi, 0\n\ts_nop 0\n\tglobal_load_lds_dwordx4 %9, %6\n\t"
               "s_mov_b64 exec, %1\n\ts_mov_b32 m0, %0"
               : "=&s"(keep), "=&s"(ex) : "s"(xb), "v"(xo0), "v"(xo1), "s"(xd), "s"(wb), "v"(wo0), "v"(wo1), "v"(wo2), "s"(wd) : "memory", "scc");
}
#define SCHED() __builtin_amdgcn_sched_barrier(0)
#define ABL_ST(x) x
#define ABL_C(x) x
#define WAITV(n) asm volatile("s_waitcnt vmcnt(" #n ")" ::: "memory")
#define WAITL0() asm volatile("s_waitcnt lgkmcnt(0)" ::: "memory")
#define BAR() __builtin_amdgcn_s_barrier()
#define LDU16(dst, ptr) asm volatile("global_load_ushort %0, %1, off" : "=v"(dst) : "v"(ptr) : "memory")
#define LDU32(dst, ptr) asm volatile("global_load_dword %0, %1, off" : "=v"(dst) : "v"(ptr) : "memory")
#define LDU64(dst, ptr) asm volatile("global_load_dwordx2 %0, %1, off" : "=v"(dst) : "v"(ptr) : "memory")
#define LDU64SC1(dst, ptr) asm volatile("global_load_dwordx2 %0, %1, off sc1" : "=v"(dst) : "v"(ptr) : "memory")

#define STAMP(k) do {} while (0)
#define LST(i) do {} while (0)
#define LSTW() do {} while (0)
#define STAMPV(k) do {} while (0)
#define STAMPW(k) do {} while (0)

__device__ __forceinline__ u64 mix64(u64 z) {
  z += 0x9E3779B97F4A7C15ull; z = (z ^ (z >> 30)) * 0xBF58476D1CE4E5B9ull; z = (z ^ (z >> 27)) * 0x94D049BB133111EBull; return z ^ (z >> 31);
}
__device__ __forceinline__ void poll_flags(const GAS u64* f, u64 tag) {
#pragma nounroll
  for (unsigned spins = 0; spins < SPIN_CAP; ++spins) {
    const u64 v = __hip_atomic_load(f, RLX_AGENT);
    if (__all(v == tag)) break;
    __builtin_amdgcn_s_sleep(1);
  }
  __builtin_amdgcn_fence(__ATOMIC_ACQUIRE, "wavefront");
}
__device__ __forceinline__ void lds_signal(LAS unsigned* c, int lane) {
  WAITL0();
  if (lane == 0) __hip_atomic_fetch_add(c, 1u, RLX_WG);
}
__device__ __forceinline__ void lds_wait(LAS unsigned* c, unsigned want) {
#pragma nounroll
  for (unsigned spins = 0; spins < SPIN_CAP * 16; ++spins) {
    if (__hip_atomic_load(c, RLX_WG) >= want) break;
    __builtin_amdgcn_s_sleep(1);
  }
  asm volatile("" ::: "memory");
}

template <int NK, int DB, int DA>
__device__ __forceinline__ void stream_gemm(__amdgpu_buffer_rsrc_t rs, int boff, const int (&aoff)[4], v4u (&bw)[DB][5], f32x4 (&acc)[5][4]) {
  v4u aw[DA][4];
#pragma unroll
  for (int k = 0; k < DA; ++k)
#pragma unroll
    for (int i = 0; i < 4; ++i) aw[k][i] = __builtin_amdgcn_raw_buffer_load_b128(rs, aoff[i] + k * 4096, 0, AUX_SC1);
  __builtin_amdgcn_sched_barrier(0);
#pragma unroll
  for (int k = 0; k < NK; ++k) {
#pragma unroll
    for (int n = 0; n < 5; ++n)
#pragma unroll
      for (int i = 0; i < 4; ++i) acc[n][i] = mfma16(bw[k % DB][n], aw[k % DA][i], acc[n][i]);
    __builtin_amdgcn_sched_barrier(0);
    if (k + DA < NK) {
#pragma unroll
      for (int i = 0; i < 4; ++i) aw[k % DA][i] = __builtin_amdgcn_raw_buffer_load_b128(rs, aoff[i] + (k + DA) * 4096, 0, AUX_SC1);
    }
    if (k + DB < NK) {
#pragma unroll
      for (int n = 0; n < 5; ++n) bw[k % DB][n] = __builtin_amdgcn_raw_buffer_load_b128(rs, boff + ((k + DB) * 5 + n) * 1024, 0, AUX_SC1);
    }
    __builtin_amdgcn_sched_barrier(0);
  }
}

constexpr int DBF = 4, DAF = 3, DBY = 4, DAY = 3;

__global__ __launch_bounds__(NTHR, 2) void s5_fused(const u16* __restrict__ x, const u16* __restrict__ lre, const u16* __restrict__ lim,
    const u16* __restrict__ Bre, const u16* __restrict__ Bim, const u16* __restrict__ Cre, const u16* __restrict__ Cim,
    const u16* __restrict__ logdt, const u16* __restrict__ Dsk, const u16* __restrict__ w1, const u16* __restrict__ b1,
    const u16* __restrict__ w2, const u16* __restrict__ b2, float* __restrict__ out, unsigned char* __restrict__ ws) {
  extern __shared__ __attribute__((aligned(16))) unsigned char smem_[];
  LAS unsigned char* sm = (LAS unsigned char*)smem_;
  const int tid = threadIdx.x, lane = tid & 63;
  const int wv = __builtin_amdgcn_readfirstlane(tid >> 6), w = wv & 3;
  const int b = blockIdx.x, xcd = b & 7, idx = b >> 3;
  const int r = 4 * (xcd >> 1) + (idx & 3), jj = idx >> 2, j = 8 * (xcd & 1) + jj;
  const int half2 = jj & 1, ct = 4 * (xcd & 1) + (jj >> 1), rt64 = 2 * r + half2;
  const __amdgpu_buffer_rsrc_t rs = __builtin_amdgcn_make_buffer_rsrc((void*)ws, 0, WS_BYTES, 0x00020000);
  LAS unsigned* cnt = (LAS unsigned*)(sm + CNT);
  STAMP(0);
  if (tid < 8) cnt[tid] = 0u;
  const int u = b * 4 + w;
  const int xrow = 16 * wv + (lane >> 3);
  const int xf0 = (((xrow >> 3) & 3) << 1) | ((xrow >> 1) & 1), xf1 = ((((xrow + 8) >> 3) & 3) << 1) | ((xrow >> 1) & 1);
  const unsigned xo0 = (unsigned)(128 * r + xrow) * 1280u + (((lane & 7) ^ xf0) << 4);
  const unsigned xo1 = (unsigned)(128 * r + xrow + 8) * 1280u + (((lane & 7) ^ xf1) << 4);
  const unsigned smb = (unsigned)(size_t)sm;
  const unsigned xdst = smb + XS + 16 * wv * 128;

  if (wv < 4) {
    v2u ep;
    LDU64(ep, ws + EPOCH_OFF);
    const int fu = (u < 800 ? u : u - 800) * 2;
    const int pu = fu >> 1, ctc = pu / 100, rem = pu % 100, ksp = rem / 5, ntc = rem % 5;
    const int n16 = lane & 15, g = lane >> 4;
    const size_t crow = (size_t)(80 * ctc + 16 * ntc + n16) * P + 32 * ksp + 4 * g;
    v2u cr0, ci0, cr1, ci1;
    LDU64(cr0, Cre + crow); LDU64(ci0, Cim + crow); LDU64(cr1, Cre + crow + 16); LDU64(ci1, Cim + crow + 16);
    unsigned plr, pli, pdt;
    { const int pc = 40 * j + (lane < 40 ? lane : 39);
      LDU16(plr, lre + pc); LDU16(pli, lim + pc); LDU16(pdt, logdt + pc); }
    unsigned bo[3];
#pragma unroll
    for (int q3 = 0; q3 < 3; ++q3) {
      int ch = 64 * q3 + lane; if (ch > 159) ch = 159;
      const int cl = 20 * (w & 1) + (ch >> 3), cs_ = ch & 7;
      bo[q3] = (unsigned)(40 * j + cl) * 1280u + ((cs_ ^ ((((cl >> 1) & 3) << 1) | (w >> 1))) << 4);
    }
    const unsigned char* bmat = (const unsigned char*)((w >> 1) ? Bim : Bre);
    const unsigned bdst = smb + BWS + 160 * w * 16;
#define STAGE_B(kt) stage5h((const unsigned char*)x + (kt) * 128, xo0, xo1, xdst + ((kt) & 3) * STAGE, bmat + (kt) * 128, bo[0], bo[1], bo[2], bdst + ((kt) & 3) * STAGE)
    STAMPV(19);
    STAGE_B(0); STAGE_B(1); STAGE_B(2); STAGE_B(3);
    STAMPV(20);
    const int rho = n16;
    const int Rrow = 32 * w + 8 * (rho >> 2) + (rho & 3);
    const int fx = ((rho >> 2) << 1) | ((rho >> 1) & 1);
    const int xa0 = XS + Rrow * 128 + ((g ^ fx) << 4);
    const int wb0 = BWS + ((rho >> 1) + 40 * (rho & 1)) * 128 + ((g ^ (((rho >> 2) << 1) | (rho & 1))) << 4);
    f32x4 acc[2][5];
#pragma unroll
    for (int i = 0; i < 2; ++i)
#pragma unroll
      for (int t = 0; t < 5; ++t) acc[i][t] = f32x4{0.f, 0.f, 0.f, 0.f};
    float cs[5] = {0.f, 0.f, 0.f, 0.f, 0.f};
    v4u fa[2][2], fb[2][5];
#define RD_B(buf, st, kk) do { LAS unsigned char* sb_ = sm + ((st) & 3) * STAGE; \
      _Pragma("unroll") for (int i = 0; i < 2; ++i) fa[buf][i] = *(LAS v4u*)(sb_ + (xa0 ^ ((kk) * 64)) + i * 512); \
      _Pragma("unroll") for (int t = 0; t < 5; ++t) fb[buf][t] = *(LAS v4u*)(sb_ + (wb0 ^ ((kk) * 64)) + t * 1024); SCHED(); } while (0)
#define MM_B(buf, sumcond) do { \
      _Pragma("unroll") for (int t = 0; t < 5; ++t) _Pragma("unroll") for (int i = 0; i < 2; ++i) acc[i][t] = mfma16(fa[buf][i], fb[buf][t], acc[i][t]); \
      if (sumcond) { _Pragma("unroll") for (int t = 0; t < 5; ++t) cs[t] = sum8(fb[buf][t], cs[t]); } SCHED(); } while (0)
    asm volatile("s_waitcnt vmcnt(15)" : "+v"(ep), "+v"(cr0), "+v"(ci0), "+v"(cr1), "+v"(ci1), "+v"(plr), "+v"(pli), "+v"(pdt) :: "memory");
    STAMPV(21);
    const u64 tag = mix64(((u64)__builtin_amdgcn_readfirstlane(ep.y) << 32) | (unsigned)__builtin_amdgcn_readfirstlane(ep.x));
    const GAS u64* wflag = (const GAS u64*)(ws + FLAG_W_OFF) + (ct * 88 + 10 * w) * 5 + (lane < 50 ? lane : 49);
    BAR();
    STAMPV(9);
    RD_B(0, 0, 0);
#pragma nounroll
    for (int it = 0; it < 9; ++it) {
      ABL_C(RD_B(1, it, 1));
      ABL_C(MM_B(0, w == 2 * (it & 1)));
      WAITL0();
      if (it < 7) WAITV(10); else if (it == 7) WAITV(5); else WAITV(0);
      BAR();
      LST(it);
      if (it < 6) ABL_ST(STAGE_B(it + 4));
      ABL_C(RD_B(0, it + 1, 0));
      ABL_C(MM_B(1, w == 1 + 2 * (it & 1)));
      if (it == 0) {
        v4u o0, o1;
        o0.x = __builtin_amdgcn_perm(ci0.x, cr0.x, 0x05040100u); o0.y = __builtin_amdgcn_perm(ci0.x, cr0.x, 0x07060302u);
        o0.z = __builtin_amdgcn_perm(ci0.y, cr0.y, 0x05040100u); o0.w = __builtin_amdgcn_perm(ci0.y, cr0.y, 0x07060302u);
        o1.x = __builtin_amdgcn_perm(ci1.x, cr1.x, 0x05040100u); o1.y = __builtin_amdgcn_perm(ci1.x, cr1.x, 0x07060302u);
        o1.z = __builtin_amdgcn_perm(ci1.y, cr1.y, 0x05040100u); o1.w = __builtin_amdgcn_perm(ci1.y, cr1.y, 0x07060302u);
        const int fo = WT_OFF + ((ctc * 88 + 2 * ksp) * 5 + ntc) * 1024 + lane * 16;
        __builtin_amdgcn_raw_buffer_store_b128(o0, rs, fo, 0, AUX_SC1);
        __builtin_amdgcn_raw_buffer_store_b128(o1, rs, fo + 5 * 1024, 0, AUX_SC1);
        if (w == 3) {
          const float dt = rbf(__expf(bf2f(pdt)));
          const float lr = bf2f(plr), li = bf2f(pli);
          const float mag = __expf(dt * lr);
          float rev = dt * li * 0.15915494309189535f; rev -= floorf(rev);
          const float ar = mag * __builtin_amdgcn_cosf(rev), ai = mag * __builtin_amdgcn_sinf(rev);
          const float nr = ar - 1.f, den = 1.f / (lr * lr + li * li);
          const float cr = (nr * lr + ai * li) * den, ci = (ai * lr - nr * li) * den;
          float pr = ar, pi = ai;
#pragma unroll
          for (int s = 0; s < 3; ++s) { const float t = pr * pr - pi * pi; pi = 2.f * pr * pi; pr = t; }
          const float p8r = pr, p8i = pi;
#pragma unroll
          for (int s = 0; s < 4; ++s) { const float t = pr * pr - pi * pi; pi = 2.f * pr * pi; pr = t; }
          if (lane < 40) {
            LAS float* pm = (LAS float*)(sm + PRM) + lane;
            pm[0] = ar; pm[40] = ai; pm[80] = p8r; pm[120] = p8i; pm[160] = pr; pm[200] = pi; pm[240] = cr; pm[280] = ci;
          }
        }
        SCHED();
      }
      if (it == 3) {
        if (lane < 2) __hip_atomic_store((GAS u64*)(ws + FLAG_W_OFF) + (ctc * 88 + 2 * ksp + lane) * 5 + ntc, tag, RLX_AGENT);
        SCHED();
      }
    }
    v2u wfl;
    LDU64SC1(wfl, wflag);
    RD_B(1, 9, 1);
    MM_B(0, w == 2);
    MM_B(1, w == 3);
    STAMP(1); STAMPW(9); LSTW(); STAMPW(19); STAMPW(20); STAMPW(21);
#pragma unroll
    for (int t = 0; t < 5; ++t) {
      cs[t] += __shfl_xor(cs[t], 16); cs[t] += __shfl_xor(cs[t], 32);
      if (lane < 16) ((LAS float*)(sm + CSB))[80 * w + 16 * t + lane] = cs[t];
    }
    WAITL0();
    BAR();
    asm volatile("s_waitcnt vmcnt(0)" : "+v"(wfl) :: "memory");
    const int q = g, c = n16;
    float uu[5][8], lr_[5], ls_[5], e[5];
    {
      const f32x4 rsa = *(LAS f32x4*)(sm + ST_RS + (32 * w + 8 * q) * 4), rsb = *(LAS f32x4*)(sm + ST_RS + (32 * w + 8 * q + 4) * 4);
      const f32x4 mba = *(LAS f32x4*)(sm + ST_MB + (32 * w + 8 * q) * 4), mbb = *(LAS f32x4*)(sm + ST_MB + (32 * w + 8 * q + 4) * 4);
      LAS float* pm = (LAS float*)(sm + PRM) + (c >> 1);
#pragma unroll
      for (int t = 0; t < 5; ++t) {
        LAS float* cp = (LAS float*)(sm + CSB) + 16 * t + c;
        const float S = (cp[0] + cp[80]) + (cp[160] + cp[240]);
        lr_[t] = pm[8 * t]; const float li = pm[40 + 8 * t]; ls_[t] = (c & 1) ? li : -li;
#pragma unroll
        for (int rr = 0; rr < 4; ++rr) {
          uu[t][rr] = rsa[rr] * (acc[0][t][rr] - mba[rr] * S);
          uu[t][4 + rr] = rsb[rr] * (acc[1][t][rr] - mbb[rr] * S);
        }
        e[t] = uu[t][0];
      }
#pragma unroll
      for (int s = 1; s < 8; ++s)
#pragma unroll
        for (int t = 0; t < 5; ++t) e[t] = lr_[t] * e[t] + (ls_[t] * swp1(e[t]) + uu[t][s]);
    }
    LAS float* es = (LAS float*)(sm + ESR);
#pragma unroll
    for (int t = 0; t < 5; ++t) es[(4 * w + q) * 80 + 16 * t + c] = e[t];
    lds_signal(cnt + 0, lane);
    if (w < 2) {
      const int cc = 40 * w + (lane < 40 ? lane : 38 + (lane & 1));
      LAS float* pm = (LAS float*)(sm + PRM) + (cc >> 1);
      const float l8r = pm[80], l8i = pm[120], lKr = pm[160], lKi = pm[200];
      const float l8s = (cc & 1) ? l8i : -l8i, lKs = (cc & 1) ? lKi : -lKi;
      lds_wait(cnt + 0, 4u);
      float ev[16];
#pragma unroll
      for (int s = 0; s < 16; ++s) ev[s] = es[s * 80 + cc];
      float E = ev[0];
#pragma unroll
      for (int s = 1; s < 16; ++s) E = l8r * E + (l8s * swp1(E) + ev[s]);
      const unsigned tlo = (unsigned)(tag >> 16) << 16, thi = (unsigned)(tag >> 32);
      if (r < 15 && lane < 40) {
        const unsigned eb = __builtin_bit_cast(unsigned, E);
        v4u o; o.x = tlo | (eb & 0xffffu); o.y = thi; o.z = tlo | (eb >> 16); o.w = thi;
        __builtin_amdgcn_raw_buffer_store_b128(o, rs, EG_OFF + (r * 1280 + 80 * j + cc) * 16, 0, AUX_SC1);
      }
      STAMP(2);
      float H = 0.f;
      if (r > 0) {
        const int eo = EG_OFF + (80 * j + cc) * 16;
#pragma nounroll
        for (unsigned spins = 0; spins < SPIN_CAP; ++spins) {
          v4u gv[15];
#pragma unroll
          for (int rp = 0; rp < 15; ++rp) gv[rp] = __builtin_amdgcn_raw_buffer_load_b128(rs, eo + (rp < r ? rp : r - 1) * (1280 * 16), 0, AUX_SC1);
          bool ok = true; H = 0.f;
#pragma unroll
          for (int rp = 0; rp < 15; ++rp) {
            ok = ok && ((gv[rp].x & 0xffff0000u) == tlo) && ((gv[rp].z & 0xffff0000u) == tlo) && (gv[rp].y == thi) && (gv[rp].w == thi);
            const float Ev = __builtin_bit_cast(float, (gv[rp].x & 0xffffu) | (gv[rp].z << 16));
            const float Hn = lKr * H + (lKs * swp1(H) + Ev);
            H = rp < r ? Hn : H;
          }
          if (__all(ok)) break;
          __builtin_amdgcn_s_sleep(1);
        }
      }
      STAMP(3);
#pragma unroll
      for (int s = 0; s < 16; ++s) { if (lane < 40) es[s * 80 + cc] = H; H = l8r * H + (l8s * swp1(H) + ev[s]); }
      lds_signal(cnt + 1, lane);
    }
    lds_wait(cnt + 1, 2u);
    {
      LAS unsigned char* scr = sm + YREG + w * 20480;
      LAS float* pm = (LAS float*)(sm + PRM) + (c >> 1);
#pragma unroll
      for (int t = 0; t < 5; ++t) {
        float h = es[(4 * w + q) * 80 + 16 * t + c];
        const float cr = pm[240 + 8 * t], ci = pm[280 + 8 * t];
        const float ca = (c & 1) ? -2.f * cr : 2.f * cr, cb = -2.f * ci;
        float hp = swp1(h), o[8];
#pragma unroll
        for (int s = 0; s < 8; ++s) {
          h = lr_[t] * h + (ls_[t] * hp + uu[t][s]);
          hp = swp1(h);
          o[s] = ca * h + cb * hp;
        }
        v4u ov; ov.x = pk2(o[0], o[1]); ov.y = pk2(o[2], o[3]); ov.z = pk2(o[4], o[5]); ov.w = pk2(o[6], o[7]);
        *(LAS v4u*)(scr + (16 * t + c) * 80 + q * 16) = ov;
      }
      WAITL0();
      const int qq = (lane & 15) >> 2, pp = lane & 3;
#pragma unroll
      for (int m = 0; m < 5; ++m) {
        const int unit = 4 * m + g, cch = unit >> 1, th = unit & 1;
        LAS unsigned char* ra = scr + (8 * cch + qq) * 80 + (16 * th + 4 * pp) * 2;
        const v2u lo = trread(ra), hi = trread(ra + 4 * 80);
        v4u ov; ov.x = lo.x; ov.y = lo.y; ov.z = hi.x; ov.w = hi.y;
        const int kap = 10 * j + cch;
        const int off = HT_OFF + (((2 * r + (w >> 1)) * 40 + (kap >> 2)) * 4 + 2 * (w & 1) + th) * 1024 + (kap & 3) * 256 + n16 * 16;
        __builtin_amdgcn_raw_buffer_store_b128(ov, rs, off, 0, AUX_SC1);
      }
    }
    STAMP(4);
    if (!__all((((u64)wfl.y << 32) | wfl.x) == tag)) poll_flags(wflag, tag);
    const int boff = WT_OFF + (ct * 88 + 10 * w) * 5 * 1024 + lane * 16;
    v4u bw[DBY][5];
#pragma unroll
    for (int k = 0; k < DBY; ++k)
#pragma unroll
      for (int n = 0; n < 5; ++n) bw[k][n] = __builtin_amdgcn_raw_buffer_load_b128(rs, boff + (k * 5 + n) * 1024, 0, AUX_SC1);
    __builtin_amdgcn_sched_barrier(0);
    WAITV(20);
    static_assert(DBY * 5 == 20, "");
    if (lane == 0) __hip_atomic_store((GAS u64*)(ws + FLAG_H_OFF) + ((r * 2 + (w >> 1)) * 16 + j) * 2 + (w & 1), tag, RLX_AGENT);
    STAMP(5);
    poll_flags((const GAS u64*)(ws + FLAG_H_OFF) + ((r * 2 + half2) * 16 + 4 * w) * 2 + (lane & 7), tag);
    STAMP(6);
    f32x4 ac2[5][4];
#pragma unroll
    for (int n = 0; n < 5; ++n)
#pragma unroll
      for (int i = 0; i < 4; ++i) ac2[n][i] = f32x4{0.f, 0.f, 0.f, 0.f};
    int aoff[4];
#pragma unroll
    for (int i = 0; i < 4; ++i) aoff[i] = HT_OFF + ((rt64 * 40 + 10 * w) * 4 + i) * 1024 + lane * 16;
    stream_gemm<10, DBY, DAY>(rs, boff, aoff, bw, ac2);
    STAMP(7);
    {
      LAS unsigned char* pr = sm + YREG + w * 20480 + lane * 16;
#pragma unroll
      for (int i = 0; i < 4; ++i)
#pragma unroll
        for (int n = 0; n < 5; ++n) *(LAS f32x4*)(pr + (i * 5 + n) * 1024) = ac2[n][i];
      lds_signal(cnt + 3, lane);
    }
    if (b == 0 && w == 0) {
#pragma nounroll
      for (int k = 0; k < 16; ++k) poll_flags((const GAS u64*)(ws + FLAG_F_OFF) + k * 64 + lane, tag);
      if (lane == 0) __hip_atomic_store((GAS u64*)(ws + EPOCH_OFF), tag, RLX_AGENT);
    }
    STAMP(8);
  } else {
    v2u ep;
    LDU64(ep, ws + EPOCH_OFF);
    const int pu = (u < 960 ? u : u - 960), ctc = pu / 120, rem = pu % 120, ksp = rem / 5, ntc = rem % 5;
    const int n16 = lane & 15, g = lane >> 4;
    unsigned wv2[16];
    { const u16* wp = w2 + (size_t)(64 * ksp + 16 * (g >> 1) + 4 * (g & 1)) * D + 80 * ctc + 16 * ntc + n16;
#pragma unroll
      for (int e = 0; e < 16; ++e) LDU16(wv2[e], wp + (size_t)(32 * (e >> 3) + 8 * ((e >> 2) & 1) + (e & 3)) * D); }
    unsigned b1v;
    LDU32(b1v, b1 + 96 * j + 2 * (lane < 48 ? lane : 47));
    unsigned wo[3];
#pragma unroll
    for (int q3 = 0; q3 < 3; ++q3) {
      const int ch = 192 * w + 64 * q3 + lane;
      wo[q3] = (unsigned)(ch / 12) * 3072u + j * 192 + (ch % 12) * 16;
    }
    const unsigned wdst = smb + W1S + 192 * w * 16;
#define STAGE_F(kt) stage5((const unsigned char*)x + (kt) * 128, xo0, xo1, xdst + ((kt) & 3) * STAGE, (const unsigned char*)w1 + (size_t)(kt) * (64 * 3072), wo[0], wo[1], wo[2], wdst + ((kt) & 3) * STAGE)
    STAMPV(19);
    STAGE_F(0); STAGE_F(1); STAGE_F(2); STAGE_F(3);
    STAMPV(20);
    const int t32 = lane & 31, hh = lane >> 5;
    const int fx = (((t32 >> 3) & 3) << 1) | ((t32 >> 1) & 1);
    const int xa0 = XS + (32 * w + t32) * 128 + ((hh ^ fx) << 4);
    const int wa0 = W1S + (8 * hh + ((lane & 15) >> 2)) * 192 + (16 * ((lane >> 4) & 1) + 4 * (lane & 3)) * 2;
    f32x16 acc[3];
#pragma unroll
    for (int n = 0; n < 3; ++n)
#pragma unroll
      for (int e = 0; e < 16; ++e) acc[n][e] = 0.f;
    float s1 = 0.f, s2 = 0.f, cs[3] = {0.f, 0.f, 0.f};
    v4u xf[2], wf[2][3];
#define RD_F(buf, st, s) do { LAS unsigned char* sb_ = sm + ((st) & 3) * STAGE; \
      xf[buf] = *(LAS v4u*)(sb_ + (xa0 ^ ((s) * 32))); \
      _Pragma("unroll") for (int n = 0; n < 3; ++n) { \
        const v2u lo = trread(sb_ + wa0 + (s) * 3072 + n * 64), hi = trread(sb_ + wa0 + (s) * 3072 + 768 + n * 64); \
        wf[buf][n].x = lo.x; wf[buf][n].y = lo.y; wf[buf][n].z = hi.x; wf[buf][n].w = hi.y; } SCHED(); } while (0)
#define MM_F(buf, s) do { \
      _Pragma("unroll") for (int n = 0; n < 3; ++n) acc[n] = mfma32(wf[buf][n], xf[buf], acc[n]); \
      s1 = sum8(xf[buf], s1); \
      s2 = dot2(xf[buf].x, xf[buf].x, s2); s2 = dot2(xf[buf].y, xf[buf].y, s2); s2 = dot2(xf[buf].z, xf[buf].z, s2); s2 = dot2(xf[buf].w, xf[buf].w, s2); \
      asm volatile("" : "+v"(s1), "+v"(s2)); \
      if (w == (s)) { _Pragma("unroll") for (int n = 0; n < 3; ++n) cs[n] = sum8(wf[buf][n], cs[n]); } SCHED(); } while (0)
    asm volatile("s_waitcnt vmcnt(15)" : "+v"(ep), "+v"(b1v), "+v"(wv2[0]), "+v"(wv2[1]), "+v"(wv2[2]), "+v"(wv2[3]), "+v"(wv2[4]), "+v"(wv2[5]), "+v"(wv2[6]), "+v"(wv2[7]),
                 "+v"(wv2[8]), "+v"(wv2[9]), "+v"(wv2[10]), "+v"(wv2[11]), "+v"(wv2[12]), "+v"(wv2[13]), "+v"(wv2[14]), "+v"(wv2[15]) :: "memory");
    STAMPV(21);
    const u64 tag = mix64(((u64)__builtin_amdgcn_readfirstlane(ep.y) << 32) | (unsigned)__builtin_amdgcn_readfirstlane(ep.x));
    const GAS u64* wflag = (const GAS u64*)(ws + FLAG_W_OFF) + (ct * 88 + 40 + 12 * w) * 5 + (lane < 60 ? lane : 59);
    BAR();
    STAMPV(9);
    RD_F(0, 0, 0);
#pragma nounroll
    for (int it = 0; it < 9; ++it) {
      ABL_C(RD_F(1, it, 1)); ABL_C(MM_F(0, 0));
      ABL_C(RD_F(0, it, 2)); ABL_C(MM_F(1, 1));
      ABL_C(RD_F(1, it, 3)); ABL_C(MM_F(0, 2));
      WAITL0();
      if (it < 7) WAITV(10); else if (it == 7) WAITV(5); else WAITV(0);
      BAR();
      LST(it);
      if (it < 6) ABL_ST(STAGE_F(it + 4));
      ABL_C(RD_F(0, it + 1, 0)); ABL_C(MM_F(1, 3));
      if (it == 0) {
        v4u o0, o1;
        o0.x = wv2[0] | (wv2[1] << 16); o0.y = wv2[2] | (wv2[3] << 16); o0.z = wv2[4] | (wv2[5] << 16); o0.w = wv2[6] | (wv2[7] << 16);
        o1.x = wv2[8] | (wv2[9] << 16); o1.y = wv2[10] | (wv2[11] << 16); o1.z = wv2[12] | (wv2[13] << 16); o1.w = wv2[14] | (wv2[15] << 16);
        const int fo = WT_OFF + ((ctc * 88 + 40 + 2 * ksp) * 5 + ntc) * 1024 + lane * 16;
        __builtin_amdgcn_raw_buffer_store_b128(o0, rs, fo, 0, AUX_SC1);
        __builtin_amdgcn_raw_buffer_store_b128(o1, rs, fo + 5 * 1024, 0, AUX_SC1);
        if (w == 0 && lane < 48) { f32x2 bb = {bf2f(b1v & 0xffffu), bfhi(b1v)}; *(LAS f32x2*)(sm + B1F + lane * 8) = bb; }
        SCHED();
      }
      if (it == 3) {
        if (lane < 2) __hip_atomic_store((GAS u64*)(ws + FLAG_W_OFF) + (ctc * 88 + 40 + 2 * ksp + lane) * 5 + ntc, tag, RLX_AGENT);
        SCHED();
      }
    }
    v2u wfl;
    LDU64SC1(wfl, wflag);
    RD_F(1, 9, 1); MM_F(0, 0);
    RD_F(0, 9, 2); MM_F(1, 1);
    RD_F(1, 9, 3); MM_F(0, 2);
    MM_F(1, 3);
    STAMP(1); STAMPW(9); LSTW(); STAMPW(19); STAMPW(20); STAMPW(21);
    s1 += __shfl_xor(s1, 32); s2 += __shfl_xor(s2, 32);
    const float mean = s1 * (1.f / D);
    const float var = fmaxf(s2 * (1.f / D) - mean * mean, 0.f);
    const float mb = rbf(mean);
    const float rsd = 1.f / rbf(sqrtf(rbf(rbf(var) + 1e-5f)));
#pragma unroll
    for (int n = 0; n < 3; ++n) cs[n] += __shfl_xor(cs[n], 32);
    if (lane < 32) {
      ((LAS float*)(sm + ST_RS))[32 * w + lane] = rsd; ((LAS float*)(sm + ST_MB))[32 * w + lane] = mb;
#pragma unroll
      for (int n = 0; n < 3; ++n) ((LAS float*)(sm + CSF))[96 * w + 32 * n + lane] = cs[n];
    }
    WAITL0();
    BAR();
    if (lane < 48) {
      LAS f32x2* cp = (LAS f32x2*)(sm + CSF) + lane;
      ((LAS f32x2*)(sm + CSW + w * 384))[lane] = (cp[0] + cp[48]) + (cp[96] + cp[144]);
    }
    asm volatile("s_waitcnt vmcnt(0)" : "+v"(wfl) :: "memory");
    if (!__all((((u64)wfl.y << 32) | wfl.x) == tag)) poll_flags(wflag, tag);
    const int boff = WT_OFF + (ct * 88 + 40 + 12 * w) * 5 * 1024 + lane * 16;
    v4u bw[DBF][5];
#pragma unroll
    for (int k = 0; k < DBF; ++k)
#pragma unroll
      for (int n = 0; n < 5; ++n) bw[k][n] = __builtin_amdgcn_raw_buffer_load_b128(rs, boff + (k * 5 + n) * 1024, 0, AUX_SC1);
    __builtin_amdgcn_sched_barrier(0);
    {
      const float nm = -rsd * mb;
      const int hoff = H1T_OFF + (((2 * r + (w >> 1)) * 48 + 3 * j) * 4 + 2 * (w & 1) + (t32 >> 4)) * 1024 + hh * 256 + (t32 & 15) * 16;
#pragma unroll
      for (int n = 0; n < 3; ++n)
#pragma unroll
        for (int hf = 0; hf < 2; ++hf) {
          float gv[8];
#pragma unroll
          for (int rq = 0; rq < 2; ++rq) {
            const int nb = 32 * n + 8 * (2 * hf + rq) + 4 * hh;
            const f32x4 S = *(LAS f32x4*)(sm + CSW + w * 384 + nb * 4), bb = *(LAS f32x4*)(sm + B1F + nb * 4);
#pragma unroll
            for (int e = 0; e < 4; ++e) {
              const float v = rsd * acc[n][8 * hf + 4 * rq + e] + (nm * S[e] + bb[e]);
              const float z = v * (-2.302208198f - 0.1029432397f * v * v);
              gv[4 * rq + e] = v * __builtin_amdgcn_rcpf(1.f + __builtin_amdgcn_exp2f(z));
            }
          }
          v4u ov; ov.x = pk2(gv[0], gv[1]); ov.y = pk2(gv[2], gv[3]); ov.z = pk2(gv[4], gv[5]); ov.w = pk2(gv[6], gv[7]);
          __builtin_amdgcn_raw_buffer_store_b128(ov, rs, hoff + n * 4096 + hf * 512, 0, AUX_SC1);
        }
    }
    STAMP(4);
    WAITV(0);
    if (lane == 0) __hip_atomic_store((GAS u64*)(ws + FLAG_F_OFF) + ((r * 2 + (w >> 1)) * 16 + j) * 2 + (w & 1), tag, RLX_AGENT);
    STAMP(5);
    poll_flags((const GAS u64*)(ws + FLAG_F_OFF) + ((r * 2 + half2) * 16 + 4 * w) * 2 + (lane & 7), tag);
    STAMP(6);
    f32x4 ac2[5][4];
#pragma unroll
    for (int n = 0; n < 5; ++n)
#pragma unroll
      for (int i = 0; i < 4; ++i) ac2[n][i] = f32x4{0.f, 0.f, 0.f, 0.f};
    int aoff[4];
#pragma unroll
    for (int i = 0; i < 4; ++i) aoff[i] = H1T_OFF + ((rt64 * 48 + 12 * w) * 4 + ((i + w) & 3)) * 1024 + lane * 16;
    stream_gemm<12, DBF, DAF>(rs, boff, aoff, bw, ac2);
    STAMP(7);
    const int trow = 64 * half2 + 16 * w + n16;
    const size_t orow = (size_t)(128 * r + trow) * D + 80 * ct + 4 * g;
    v2u xv[5], dv[5], bv[5];
#pragma unroll
    for (int n = 0; n < 5; ++n) {
      xv[n] = *(const v2u*)(x + orow + 16 * n);
      dv[n] = *(const v2u*)(Dsk + 80 * ct + 4 * g + 16 * n);
      bv[n] = *(const v2u*)(b2 + 80 * ct + 4 * g + 16 * n);
    }
    {
      LAS unsigned char* pr = sm + FREG + w * 15360 + lane * 16;
#pragma unroll
      for (int i = 1; i < 4; ++i)
#pragma unroll
        for (int n = 0; n < 5; ++n) *(LAS f32x4*)(pr + ((i - 1) * 5 + n) * 1024) = ac2[n][i];
      lds_signal(cnt + 2, lane);
    }
    const float ers = ((LAS float*)(sm + ST_RS))[trow], emb = ((LAS float*)(sm + ST_MB))[trow];
    lds_wait(cnt + 2, 4u);
    f32x4 gg[5], sk[5], xx[5];
#pragma unroll
    for (int n = 0; n < 5; ++n) {
      f32x4 h2 = ac2[n][0];
#pragma unroll
      for (int o = 1; o < 4; ++o) {
        const int wp = (w + o) & 3;
        h2 += *(LAS f32x4*)(sm + FREG + wp * 15360 + ((3 - o) * 5 + n) * 1024 + lane * 16);
      }
      const float xs[4] = {bf2f(xv[n].x & 0xffffu), bfhi(xv[n].x), bf2f(xv[n].y & 0xffffu), bfhi(xv[n].y)};
      const float ds[4] = {bf2f(dv[n].x & 0xffffu), bfhi(dv[n].x), bf2f(dv[n].y & 0xffffu), bfhi(dv[n].y)};
      const float bs[4] = {bf2f(bv[n].x & 0xffffu), bfhi(bv[n].x), bf2f(bv[n].y & 0xffffu), bfhi(bv[n].y)};
#pragma unroll
      for (int e = 0; e < 4; ++e) {
        const float a = rbf(rbf(h2[e]) + bs[e]);
        gg[n][e] = rbf(a * __builtin_amdgcn_rcpf(1.f + __builtin_amdgcn_exp2f(-1.4426950408889634f * a)));
        const float xn = rbf(rbf(xs[e] - emb) * ers);
        sk[n][e] = rbf(xn * ds[e]);
        xx[n][e] = xs[e];
      }
    }
    STAMP(2);
    lds_wait(cnt + 3, 4u);
    STAMP(3);
#pragma unroll
    for (int n = 0; n < 5; ++n) {
      f32x4 y = sk[n];
#pragma unroll
      for (int o = 0; o < 4; ++o) y += *(LAS f32x4*)(sm + YREG + o * 20480 + (w * 5 + n) * 1024 + lane * 16);
      *(f32x4*)(out + orow + 16 * n) = xx[n] + y * gg[n];
    }
    STAMP(8);
  }
}
}

extern "C" void kernel_launch(void* const* d_in, const int* in_sizes, int n_in,
                              void* d_out, int out_size, void* d_ws, size_t ws_size,
                              hipStream_t stream) {
  using s5k::u16;
  (void)hipFuncSetAttribute((const void*)s5k::s5_fused, hipFuncAttributeMaxDynamicSharedMemorySize, s5k::LDS_BYTES);
  s5k::s5_fused<<<s5k::NWG, s5k::NTHR, s5k::LDS_BYTES, stream>>>(
      (const u16*)d_in[0], (const u16*)d_in[1], (const u16*)d_in[2], (const u16*)d_in[3], (const u16*)d_in[4], (const u16*)d_in[5],
      (const u16*)d_in[6], (const u16*)d_in[7], (const u16*)d_in[8], (const u16*)d_in[10], (const u16*)d_in[11], (const u16*)d_in[12],
      (const u16*)d_in[13], (float*)d_out, (unsigned char*)d_ws);
}
```

```cpp
#include <hip/hip_runtime.h>
#include <hip/hip_bf16.h>
#include <stdint.h>
#include <type_traits>

namespace s5k {
typedef unsigned short u16;
typedef unsigned long long u64;
typedef unsigned v4u __attribute__((ext_vector_type(4)));
typedef unsigned v2u __attribute__((ext_vector_type(2)));
typedef float f32x4 __attribute__((ext_vector_type(4)));
typedef float f32x2 __attribute__((ext_vector_type(2)));
typedef float f32x16 __attribute__((ext_vector_type(16)));
typedef __bf16 bf16x8 __attribute__((ext_vector_type(8)));
typedef __bf16 bf16x2 __attribute__((ext_vector_type(2)));
typedef short s16x4 __attribute__((ext_vector_type(4)));
#define GAS __attribute__((address_space(1)))
#define LAS __attribute__((address_space(3)))
#define RLX_AGENT __ATOMIC_RELAXED, __HIP_MEMORY_SCOPE_AGENT
#define RLX_WG __ATOMIC_RELAXED, __HIP_MEMORY_SCOPE_WORKGROUP

constexpr int L = 2048, D = 640, P = 640, F = 1536;
constexpr int NWG = 256, NTHR = 512;
constexpr int EPOCH_OFF = 0;
constexpr int FLAG_E_OFF = 4096;
constexpr int FLAG_F_OFF = 65536;
constexpr int FLAG_H_OFF = 65536 + 8192;
constexpr int EG_OFF = 131072;
constexpr int PRS_OFF = 512 << 10;
constexpr int PCB_OFF = 576 << 10;
constexpr int PCF_OFF = 640 << 10;
constexpr int WT_OFF = 1 << 20;
constexpr int H1T_OFF = 8 << 20;
constexpr int HT_OFF = 16 << 20;
constexpr int WS_BYTES = 24 << 20;
constexpr int XS = 0, BWS = 16384, W1S = 26624, STAGE = 38912;
constexpr int XR = 3 * STAGE;
constexpr int W2SCR = XR, W1SCR = XR + 9216, CSCR = XR + 17408;
constexpr int MISC = 155648;
constexpr int ST2 = MISC;
constexpr int CSB = MISC + 1024;
constexpr int CSF = MISC + 2048;
constexpr int B1F = MISC + 2432;
constexpr int PRM = MISC + 2816;
constexpr int CNT = MISC + 4096;
constexpr int TAGL = MISC + 4128;
constexpr int DB2 = MISC + 4160;
constexpr int HIN = DB2 + 640;
constexpr int LSTAMP = MISC + 5120;
constexpr int LDS_BYTES = MISC + 8192;
constexpr int SLOT0 = 40960;
constexpr int ESR = SLOT0 + 20 * 5120;
__device__ __forceinline__ int yslot(int w, int k) { return SLOT0 + (k ? 11 + w : (w == 3 ? 19 : 4 + w)) * 5120; }
__device__ __forceinline__ int fslot(int w, int k) { return SLOT0 + (k == 0 ? 15 + w : k == 1 ? w : 7 + w) * 5120; }
static_assert(ESR + 5120 <= MISC, "");
static_assert(LDS_BYTES <= 163840, "");
constexpr unsigned SPIN_CAP = 1u << 20;
constexpr int AUX_SC1 = 16;

__device__ __forceinline__ float bf2f(unsigned b) { return __builtin_bit_cast(float, b << 16); }
__device__ __forceinline__ float bfhi(unsigned b) { return __builtin_bit_cast(float, b & 0xffff0000u); }
__device__ __forceinline__ unsigned pk2(float a, float b) { f32x2 f = {a, b}; return __builtin_bit_cast(unsigned, __builtin_convertvector(f, bf16x2)); }
__device__ __forceinline__ float rbf(float a) { return bf2f(pk2(a, a) & 0xffffu); }
__device__ __forceinline__ float dot2(unsigned a, unsigned b, float c) {
  return __builtin_amdgcn_fdot2_f32_bf16(__builtin_bit_cast(bf16x2, a), __builtin_bit_cast(bf16x2, b), c, false);
}
__device__ __forceinline__ float sum8(v4u a, float c) {
  const unsigned ones = 0x3f803f80u;
  c = dot2(a.x, ones, c); c = dot2(a.y, ones, c); c = dot2(a.z, ones, c); c = dot2(a.w, ones, c); return c;
}
__device__ __forceinline__ float sq8(v4u a, float c) {
  c = dot2(a.x, a.x, c); c = dot2(a.y, a.y, c); c = dot2(a.z, a.z, c); c = dot2(a.w, a.w, c); return c;
}
__device__ __forceinline__ float swp1(float v) {
  return __builtin_bit_cast(float, __builtin_amdgcn_update_dpp(0, __builtin_bit_cast(int, v), 0xB1, 0xF, 0xF, true));
}
template <int CTRL> __device__ __forceinline__ float dppf(float v) {
  return __builtin_bit_cast(float, __builtin_amdgcn_update_dpp(0, __builtin_bit_cast(int, v), CTRL, 0xF, 0xF, true));
}
__device__ __forceinline__ float add16(float v) {
  float c_ = v;
  asm("s_nop 1\n\tv_permlane16_swap_b32 %0, %1" : "+v"(v), "+v"(c_));
  return v + c_;
}
__device__ __forceinline__ float add32(float v) {
  float c_ = v;
  asm("s_nop 1\n\tv_permlane32_swap_b32 %0, %1" : "+v"(v), "+v"(c_));
  return v + c_;
}
__device__ __forceinline__ float sum32(float v) {
  v += dppf<0xB1>(v); v += dppf<0x4E>(v); v += dppf<0x141>(v); v += dppf<0x140>(v); return add16(v);
}
__device__ __forceinline__ f32x2 cmad(f32x2 lrr, f32x2 lsi, f32x2 h, f32x2 u) {
  return lrr * h + (lsi * __builtin_shufflevector(h, h, 1, 0) + u);
}
__device__ __forceinline__ f32x4 mfma16(v4u a, v4u b, f32x4 c) {
  return __builtin_amdgcn_mfma_f32_16x16x32_bf16(__builtin_bit_cast(bf16x8, a), __builtin_bit_cast(bf16x8, b), c, 0, 0, 0);
}
__device__ __forceinline__ f32x16 mfma32(v4u a, v4u b, f32x16 c) {
  return __builtin_amdgcn_mfma_f32_32x32x16_bf16(__builtin_bit_cast(bf16x8, a), __builtin_bit_cast(bf16x8, b), c, 0, 0, 0);
}
__device__ __forceinline__ v2u trread(LAS unsigned char* p) {
  return __builtin_bit_cast(v2u, __builtin_amdgcn_ds_read_tr16_b64_v4i16((LAS s16x4*)p));
}
__device__ __forceinline__ void dma1(const void* gb, unsigned vo, unsigned ld) {
  asm volatile("s_mov_b32 m0, %2\n\ts_nop 0\n\tglobal_load_lds_dwordx4 %1, %0" :: "s"(gb), "v"(vo), "s"(ld) : "memory");
}
__device__ __forceinline__ void dma1h(const void* gb, unsigned vo, unsigned ld) {
  asm volatile("s_mov_b32 m0, %2\n\ts_mov_b32 exec_hi, 0\n\tglobal_load_lds_dwordx4 %1, %0\n\ts_mov_b32 exec_hi, -1" :: "s"(gb), "v"(vo), "s"(ld) : "memory");
}
__device__ __forceinline__ void dma1s(const void* gb, unsigned vo, unsigned ld) {
  asm volatile("s_mov_b32 m0, %2\n\ts_nop 0\n\tglobal_load_lds_dwordx4 %1, %0 sc1" :: "s"(gb), "v"(vo), "s"(ld) : "memory");
}
template <int N> __device__ __forceinline__ void waitv() { asm volatile("s_waitcnt vmcnt(%0)" :: "i"(N) : "memory"); }
#define SCHED() __builtin_amdgcn_sched_barrier(0)
#define ABL_ST(x) x
#define WAITL0() asm volatile("s_waitcnt lgkmcnt(0)" ::: "memory")
#define BAR() __builtin_amdgcn_s_barrier()
#define LDU16(dst, ptr) asm volatile("global_load_ushort %0, %1, off" : "=v"(dst) : "v"(ptr) : "memory")
#define LDU32(dst, ptr) asm volatile("global_load_dword %0, %1, off" : "=v"(dst) : "v"(ptr) : "memory")
#define LDU64(dst, ptr) asm volatile("global_load_dwordx2 %0, %1, off" : "=v"(dst) : "v"(ptr) : "memory")
#define LDU128(dst, ptr) asm volatile("global_load_dwordx4 %0, %1, off" : "=v"(dst) : "v"(ptr) : "memory")

#define STAMP(k) do {} while (0)
#define STAMPC(k) do {} while (0)
#define TS(k) do {} while (0)
#define TP(k) do {} while (0)
#define TP(k) do {} while (0)
#define TS_OUT() do {} while (0)
#define STAMPS_OUT() do {} while (0)

__device__ __forceinline__ u64 mix64(u64 z) {
  z += 0x9E3779B97F4A7C15ull; z = (z ^ (z >> 30)) * 0xBF58476D1CE4E5B9ull; z = (z ^ (z >> 27)) * 0x94D049BB133111EBull; return z ^ (z >> 31);
}
__device__ __forceinline__ void poll_flags(const GAS u64* f, u64 tag) {
#pragma nounroll
  for (unsigned spins = 0; spins < SPIN_CAP; ++spins) {
    const u64 v = __hip_atomic_load(f, RLX_AGENT);
    if (__all(v == tag)) break;
    __builtin_amdgcn_s_sleep(1);
  }
  __builtin_amdgcn_fence(__ATOMIC_ACQUIRE, "wavefront");
}
__device__ __forceinline__ void lds_signal(LAS unsigned* c, int lane) {
  WAITL0();
  if (lane == 0) __hip_atomic_fetch_add(c, 1u, RLX_WG);
}
__device__ __forceinline__ void lds_wait(LAS unsigned* c, unsigned want) {
#pragma nounroll
  for (unsigned spins = 0; spins < SPIN_CAP * 16; ++spins) {
    if (__hip_atomic_load(c, RLX_WG) >= want) break;
    __builtin_amdgcn_s_sleep(1);
  }
  asm volatile("" ::: "memory");
}
template <int NK> __device__ __forceinline__ int rotk(int k, int s) { const int a = k + s; return a >= NK ? a - NK : a; }
__device__ __forceinline__ v4u ldsc1(__amdgpu_buffer_rsrc_t rs, int voff, int soff) { return __builtin_amdgcn_raw_buffer_load_b128(rs, voff, soff, AUX_SC1); }

template <int NK, int DB, int DA, int NL, class LW, class HK>
__device__ __forceinline__ void stream_gemm(__amdgpu_buffer_rsrc_t rs, int boff, const int (&aoff)[4], int s2, v4u (&bw)[DB][5], f32x4 (&acc)[5][4], LW lw, HK hook) {
  v4u aw[DA][4];
#pragma unroll
  for (int k = 0; k < DA; ++k) {
#pragma unroll
    for (int i = 0; i < 4; ++i) aw[k][i] = ldsc1(rs, aoff[i], rotk<NK>(k, s2) * 4096);
    SCHED();
  }
#pragma unroll
  for (int k = 0; k < NK; ++k) {
#pragma unroll
    for (int i = 0; i < 4; ++i) {
#pragma unroll
      for (int n = 0; n < 5; ++n) acc[n][i] = mfma16(bw[k % DB][n], aw[k % DA][i], acc[n][i]);
      SCHED();
      if (k + DA < NK) aw[k % DA][i] = ldsc1(rs, aoff[i], rotk<NK>(k + DA, s2) * 4096);
      SCHED();
    }
    if (k == NK - DA) hook();
    if (k + DB < NK) {
      if (k < NL) {
        LAS unsigned char* lp = lw(k);
#pragma unroll
        for (int n = 0; n < 5; ++n) bw[k % DB][n] = *(LAS v4u*)(lp + n * 1024);
      } else {
#pragma unroll
        for (int n = 0; n < 5; ++n) bw[k % DB][n] = ldsc1(rs, boff + n * 1024, rotk<NK>(k + DB, s2) * 5120);
      }
    }
    SCHED();
  }
}

#define THR 99
#define ROT1 0
#define ROT2 0
constexpr int DBF = 5, DAF = 3, DBY = 5, DAY = 3, NLF = 3, NLY = 4;
constexpr int PBF = 2, PBY = 3;

__global__ __launch_bounds__(NTHR, 2) void s5_fused(const u16* __restrict__ x, const u16* __restrict__ lre, const u16* __restrict__ lim,
    const u16* __restrict__ Bre, const u16* __restrict__ Bim, const u16* __restrict__ Cre, const u16* __restrict__ Cim,
    const u16* __restrict__ logdt, const u16* __restrict__ Dsk, const u16* __restrict__ w1, const u16* __restrict__ b1,
    const u16* __restrict__ w2, const u16* __restrict__ b2, float* __restrict__ out, unsigned char* __restrict__ ws) {
  extern __shared__ __attribute__((aligned(16))) unsigned char smem_[];
  LAS unsigned char* sm = (LAS unsigned char*)smem_;
  asm volatile("" :: "s"(x), "s"(lre), "s"(lim), "s"(Bre), "s"(Bim), "s"(Cre), "s"(Cim), "s"(logdt), "s"(Dsk), "s"(w1), "s"(b1), "s"(w2), "s"(b2), "s"(out), "s"(ws));
  const int tid = threadIdx.x, lane = tid & 63;
  const int wv = __builtin_amdgcn_readfirstlane(tid >> 6), w = wv & 3;
  const int b = blockIdx.x, xcd = b & 7, idx = b >> 3;
  const int r = 4 * (xcd >> 1) + (idx & 3), jj = idx >> 2, j = 8 * (xcd & 1) + jj;
  const int half2 = jj & 1, ct = 4 * (xcd & 1) + (jj >> 1), rt64 = 2 * r + half2;
  const int s1 = ROT1 == 1 ? (j + 3 * r) % 10 : ROT1 == 2 ? 5 * ((j + r) & 1) : ROT1 == 3 ? 3 * ((j + r) % 3) : 0;
  STAMP(0); STAMPC(22);
  TS(0);
  const unsigned smb = (unsigned)(size_t)sm;
  const __amdgpu_buffer_rsrc_t rs = __builtin_amdgcn_make_buffer_rsrc((void*)ws, 0, WS_BYTES, 0x00020000);
  LAS unsigned* cnt = (LAS unsigned*)(sm + CNT);
  const int n16 = lane & 15, g = lane >> 4;
  const int s2f = ROT2 ? 3 * (ct & 3) : 0, s2y = ROT2 ? (5 * (ct & 3)) >> 1 : 0;

  if (wv < 4) {
    unsigned so[10];
    { const int xrow = 32 * w + (lane >> 3);
      const unsigned xb = (unsigned)(128 * r + xrow) * 1280u + (((lane & 7) ^ ((xrow >> 1) & 1)) << 4);
#pragma unroll
      for (int q = 0; q < 4; ++q) so[q] = (xb + q * 10240u) ^ (q << 5); }
    const unsigned char* bmat = (const unsigned char*)((w >> 1) ? Bim : Bre);
    const unsigned l16 = lane * 16;
    auto job = [&](int d) __attribute__((always_inline)) {
      if (THR < 60 && d >= THR && d < 127) waitv<THR - 1>();
      if (d < 100) {
        const int st = d / 10, p = d % 10, kt = rotk<10>(st, s1);
        const unsigned sb = smb + (st % 3) * STAGE;
        if (p < 4) dma1((const unsigned char*)x + kt * 128, so[p], sb + XS + (32 * w + 8 * p) * 128);
        else if (p < 6) dma1(bmat + kt * 128, so[p], sb + BWS + 2560 * w + (p - 4) * 1024);
        else if (p == 6) dma1h(bmat + kt * 128, so[p], sb + BWS + 2560 * w + 2048);
        else dma1((const unsigned char*)w1 + (size_t)kt * (64 * 3072), so[p], sb + W1S + 3072 * w + (p - 7) * 1024);
      } else if (d < 102) {
        const int pc = 2 * w + (d - 100);
        dma1s(ws + (pc == 0 ? PRS_OFF + r * 1024 : pc == 1 ? PCB_OFF + j * 1024 : PCF_OFF + j * 6144 + (pc - 2) * 1024), l16,
              smb + (pc == 0 ? ST2 : pc == 1 ? CSB : ESR + (pc - 2) * 1024));
      } else if (d < 127) {
        const int sl = (d - 102) / 5, n = (d - 102) % 5;
        if (sl == 2 || sl == 4) dma1s(ws + WT_OFF + (ct * 88 + 10 * w + rotk<10>(DBY + (sl >> 2), s2y)) * 5120 + n * 1024, l16, smb + yslot(w, sl >> 2) + n * 1024);
        else { const int k = sl == 3 ? 2 : sl; dma1s(ws + WT_OFF + (ct * 88 + 40 + 12 * w + rotk<12>(DBF + k, s2f)) * 5120 + n * 1024, l16, smb + fslot(w, k) + n * 1024); }
      }
    };
    TP(1);
#pragma unroll
    for (int d = 0; d < 4; ++d) job(d);
    TP(2);
#pragma unroll
    for (int q3 = 0; q3 < 3; ++q3) {
      int ch = 64 * q3 + lane; if (ch > 159) ch = 159;
      const int cl = 20 * (w & 1) + (ch >> 3), cs_ = ch & 7;
      so[4 + q3] = (unsigned)(40 * j + cl) * 1280u + ((cs_ ^ ((cl >> 1) & 7) ^ ((w >> 1) << 2)) << 4);
      const int c2 = 192 * w + 64 * q3 + lane;
      so[7 + q3] = (unsigned)(c2 / 12) * 3072u + j * 192 + (c2 % 12) * 16;
    }
#pragma unroll
    for (int d = 4; d < 10; ++d) job(d);
    TP(3);
    v4u cre, cim;
    const int u = b * 4 + w, cu = u < 800 ? u : u - 800, cd8 = cu / 10, ccb = cu % 10;
    { const unsigned co = (unsigned)((8 * cd8 + (lane >> 3)) * P + 64 * ccb + 8 * (lane & 7)) * 2;
      asm volatile("global_load_dwordx4 %0, %2, %3\n\tglobal_load_dwordx4 %1, %2, %4" : "=&v"(cre), "=&v"(cim) : "v"(co), "s"(Cre), "s"(Cim) : "memory"); }
    job(10); job(11);
    STAMP(19);
    if (tid < 8) cnt[tid] = 0u;
    const int rho = n16;
    const int Rrow = 32 * w + 8 * (rho >> 2) + (rho & 3);
    const int fx = ((rho >> 2) << 1) | ((rho >> 1) & 1);
    const int xa0 = XS + Rrow * 128 + ((g ^ fx) << 4);
    const int wb0 = BWS + rho * 128 + ((g ^ ((rho >> 1) & 7)) << 4);
    const int wb4 = BWS + (40 * (rho & 1) + 32 + (rho >> 1)) * 128 + ((g ^ (rho >> 2) ^ ((rho & 1) << 2)) << 4);
    f32x4 acc[2][5];
#pragma unroll
    for (int i = 0; i < 2; ++i)
#pragma unroll
      for (int t = 0; t < 5; ++t) acc[i][t] = f32x4{0.f, 0.f, 0.f, 0.f};
    v4u fa[2][2][2], fb[2][2][5];
    auto rdB = [&](int set, int st, int q) __attribute__((always_inline)) {
      LAS unsigned char* sb = sm + (st % 3) * STAGE;
      const int kk = q / 7, e = q % 7;
      if (e < 2) fa[set][kk][e] = *(LAS v4u*)(sb + (xa0 ^ (kk * 64)) + e * 512);
      else if (e < 6) { const int t = e - 2; fb[set][kk][t] = *(LAS v4u*)(sb + (wb0 ^ ((kk ^ (t & 1)) * 64)) + (t & 1) * 5120 + (t >> 1) * 2048); }
      else fb[set][kk][4] = *(LAS v4u*)(sb + (wb4 ^ (kk * 64)));
    };
    auto mmB = [&](int set, int m) __attribute__((always_inline)) {
      const int kk = m / 10, t = (m % 10) >> 1, i = m & 1;
      acc[i][t] = mfma16(fa[set][kk][i], fb[set][kk][t], acc[i][t]);
    };
    TP(4);
    asm volatile("s_waitcnt vmcnt(2)" : "+v"(cre), "+v"(cim) :: "memory");
    TP(5);
#pragma unroll
    for (int d = 12; d < 20; ++d) job(d);
    TP(6);
    {
      v4u o0, o1;
      o0.x = __builtin_amdgcn_perm(cim.x, cre.x, 0x05040100u); o0.y = __builtin_amdgcn_perm(cim.x, cre.x, 0x07060302u);
      o0.z = __builtin_amdgcn_perm(cim.y, cre.y, 0x05040100u); o0.w = __builtin_amdgcn_perm(cim.y, cre.y, 0x07060302u);
      o1.x = __builtin_amdgcn_perm(cim.z, cre.z, 0x05040100u); o1.y = __builtin_amdgcn_perm(cim.z, cre.z, 0x07060302u);
      o1.z = __builtin_amdgcn_perm(cim.w, cre.w, 0x05040100u); o1.w = __builtin_amdgcn_perm(cim.w, cre.w, 0x07060302u);
      LAS unsigned char* sc = sm + CSCR + w * 2048;
      *(LAS v4u*)(sc + (2 * (lane & 7)) * 128 + (lane >> 3) * 16) = o0;
      *(LAS v4u*)(sc + (2 * (lane & 7) + 1) * 128 + (lane >> 3) * 16) = o1;
      WAITL0();
      const v4u q0 = *(LAS v4u*)(sc + lane * 16), q1 = *(LAS v4u*)(sc + 1024 + lane * 16);
      const int ctc = cd8 / 10, ntc = (cd8 % 10) >> 1;
      const int fo = WT_OFF + ((ctc * 88 + 4 * ccb + (lane >> 5)) * 5 + ntc) * 1024 + ((lane >> 3) & 3) * 256 + (8 * (cd8 & 1) + (lane & 7)) * 16;
      __builtin_amdgcn_raw_buffer_store_b128(q0, rs, fo, 0, AUX_SC1);
      __builtin_amdgcn_raw_buffer_store_b128(q1, rs, fo + 2 * 5120, 0, AUX_SC1);
    }
    TP(7);
    BAR();
    TP(8);
    STAMP(9);
#pragma unroll
    for (int q = 0; q < 14; ++q) rdB(0, 0, q);
    SCHED();
#pragma unroll
    for (int m = 0; m < 20; ++m) { mmB(0, m); if (m & 1) job(20 + (m >> 1)); SCHED(); }
    TS(1);
    waitv<12>(); TS(2); BAR(); TS(3);
#pragma unroll
    for (int q = 0; q < 14; ++q) rdB(1, 1, q);
#pragma unroll
    for (int d = 30; d < 40; ++d) job(d);
    const v2u tgl = *(LAS v2u*)(sm + TAGL);
    WAITL0(); waitv<10>(); BAR();
#pragma unroll
    for (int d = 40; d < 44; ++d) job(d);
    const u64 tag = ((u64)__builtin_amdgcn_readfirstlane(tgl.y) << 32) | (unsigned)__builtin_amdgcn_readfirstlane(tgl.x);
    v4u efv[2];
    const int boff = WT_OFF + (ct * 88 + 10 * w) * 5120 + lane * 16;
    const int s2 = s2y;
    v4u bw[DBY][5];
    auto stepB = [&](auto I_) __attribute__((always_inline)) {
      constexpr int i = decltype(I_)::value;
      auto jobA = [&](int a) __attribute__((always_inline)) {
        if (i <= 6 && a < 6) job(10 * (i + 3) + 4 + a);
        else if (i == 5 && a == 6) job(102);
        else if (i == 6 && a == 6) job(105);
        else if (i == 7 && a < 5) job(112 + a);
        else if (i == 8 && a < 4) job(123 + a);
        else if (i == 9 && a < 5) bw[1][a] = ldsc1(rs, boff + a * 1024, rotk<10>(1, s2) * 5120);
      };
      auto jobB = [&](int a) __attribute__((always_inline)) {
        if (i <= 5 && a < 4) job(10 * (i + 4) + a);
        else if (i == 4) job(100 + a - 4);
        else if (i == 5) job(103 + a - 4);
        else if (i == 6) job(106 + a);
        else if (i == 7) job(117 + a);
        else if (i == 8 && a < 5) bw[0][a] = ldsc1(rs, boff + a * 1024, s2 * 5120);
        else if (i == 9 && a < 5) bw[2][a] = ldsc1(rs, boff + a * 1024, rotk<10>(2, s2) * 5120);
      };
#pragma unroll
      for (int m = 0; m < 14; ++m) { mmB(i & 1, m); if (i < 9) rdB((i + 1) & 1, i + 1, m); if (m & 1) jobA(m >> 1); SCHED(); }
      if (i == 8) { WAITL0(); BAR(); }
      if (i < 8) {
        WAITL0();
        TS(1 + 3 * i);
        if (i < 4) waitv<10>();
        else if (i == 4) {
          asm volatile("s_waitcnt vmcnt(10)" : "+v"(efv[0]), "+v"(efv[1]) :: "memory");
          if (w == 1) {
            const unsigned tl = (unsigned)tag, th = (unsigned)(tag >> 32);
            const unsigned bad = (efv[0].x ^ tl) | (efv[0].y ^ th) | (efv[0].z ^ tl) | (efv[0].w ^ th) | (efv[1].x ^ tl) | (efv[1].y ^ th) | (efv[1].z ^ tl) | (efv[1].w ^ th);
            if (!__all(bad == 0u)) {
#pragma nounroll
              for (int k = 0; k < 4; ++k) poll_flags((const GAS u64*)(ws + FLAG_E_OFF) + k * 64 + lane, tag);
            }
            if (b == 0 && lane == 0) __hip_atomic_store((GAS u64*)(ws + EPOCH_OFF), tag, RLX_AGENT);
          }
        }
        else if (i == 5) waitv<13>();
        else if (i == 6) waitv<14>();
        else waitv<12>();
        TS(2 + 3 * i);
        BAR();
        TS(3 + 3 * i);
      }
      if (i == 1 && tid == 0) __hip_atomic_store((GAS u64*)(ws + FLAG_E_OFF) + b, tag, RLX_AGENT);
      if (i == 3) {
        asm volatile("s_cmp_eq_u32 %4, 1\n\ts_cselect_b64 exec, -1, 0\n\tglobal_load_dwordx4 %0, %2, %3 sc1\n\tglobal_load_dwordx4 %1, %2, %3 offset:16 sc1\n\ts_mov_b64 exec, -1"
                     : "=&v"(efv[0]), "=&v"(efv[1]) : "v"(l16 * 2), "s"(ws + FLAG_E_OFF), "s"(w) : "memory", "scc");
      }
      SCHED();
#pragma unroll
      for (int m = 14; m < 20; ++m) { mmB(i & 1, m); jobB(m - 14); SCHED(); }
      if (i == 6 && w == 3) {
        const int l48 = lane < 48 ? lane : 47;
        f32x2 a = *(LAS f32x2*)(sm + ESR + l48 * 8);
#pragma unroll
        for (int k = 1; k < 16; ++k) a += *(LAS f32x2*)(sm + ESR + k * 384 + l48 * 8);
        *(LAS f32x2*)(sm + CSF + l48 * 8) = a;
        SCHED();
      }
    };
#define IC(n) std::integral_constant<int, n>{}
    stepB(IC(1)); stepB(IC(2)); stepB(IC(3)); stepB(IC(4)); stepB(IC(5)); stepB(IC(6)); stepB(IC(7)); stepB(IC(8)); stepB(IC(9));
    STAMP(1); TS(25);
    const int q = g, c = n16;
    f32x2 ur[8], ui[8], lr2, li2, er, ei;
    float u4[8], lr4, ls4, e4;
    LAS float* pm = (LAS float*)(sm + PRM);
    {
      LAS f32x4* sp = (LAS f32x4*)(sm + ST2 + (32 * w + 8 * q) * 8);
      const f32x4 s0 = sp[0], s1_ = sp[1], s2_ = sp[2], s3 = sp[3];
      const f32x4 rsa = {s0[0], s0[2], s1_[0], s1_[2]}, rsb = {s2_[0], s2_[2], s3[0], s3[2]};
      const f32x4 mba = {s0[1], s0[3], s1_[1], s1_[3]}, mbb = {s2_[1], s2_[3], s3[1], s3[3]};
      f32x4 va[5], vb[5];
#pragma unroll
      for (int t = 0; t < 5; ++t) {
        const float S = ((LAS float*)(sm + CSB))[16 * t + c];
        va[t] = (acc[0][t] - mba * S) * rsa; vb[t] = (acc[1][t] - mbb * S) * rsb;
      }
      lr2 = f32x2{pm[c], pm[16 + c]}; li2 = f32x2{pm[40 + c], pm[56 + c]};
#pragma unroll
      for (int s = 0; s < 4; ++s) {
        ur[s] = f32x2{va[0][s], va[2][s]}; ui[s] = f32x2{va[1][s], va[3][s]};
        ur[4 + s] = f32x2{vb[0][s], vb[2][s]}; ui[4 + s] = f32x2{vb[1][s], vb[3][s]};
      }
      er = ur[0]; ei = ui[0];
      lr4 = pm[32 + (c >> 1)]; { const float li = pm[72 + (c >> 1)]; ls4 = (c & 1) ? li : -li; }
#pragma unroll
      for (int s = 0; s < 4; ++s) { u4[s] = va[4][s]; u4[4 + s] = vb[4][s]; }
      e4 = u4[0];
#pragma unroll
      for (int s = 1; s < 8; ++s) {
        const f32x2 nr = lr2 * er + (ur[s] - li2 * ei), ni = lr2 * ei + (li2 * er + ui[s]);
        er = nr; ei = ni;
        e4 = lr4 * e4 + (ls4 * swp1(e4) + u4[s]);
      }
    }
    LAS float* es = (LAS float*)(sm + ESR);
    es[(4 * w + q) * 80 + 2 * c] = er[0]; es[(4 * w + q) * 80 + 2 * c + 1] = ei[0];
    es[(4 * w + q) * 80 + 32 + 2 * c] = er[1]; es[(4 * w + q) * 80 + 33 + 2 * c] = ei[1];
    es[(4 * w + q) * 80 + 64 + c] = e4;
    lds_signal(cnt + 0, lane);
    waitv<15>();
    if (lane == 0) __hip_atomic_fetch_add(cnt + 4 + w, 1u, RLX_WG);
    if (w < 2) {
      const int cc = 40 * w + (lane < 40 ? lane : 38 + (lane & 1));
      const float l8r = pm[80 + (cc >> 1)], l8i = pm[120 + (cc >> 1)], lKr = pm[160 + (cc >> 1)], lKi = pm[200 + (cc >> 1)];
      const float l8s = (cc & 1) ? l8i : -l8i, lKs = (cc & 1) ? lKi : -lKi;
      lds_wait(cnt + 0, 4u);
      float ev[16];
#pragma unroll
      for (int s = 0; s < 16; ++s) ev[s] = es[s * 80 + cc];
      float E = 0.f;
#pragma unroll
      for (int s = 0; s < 16; ++s) { const float pre = E; E = l8r * E + (l8s * swp1(E) + ev[s]); ev[s] = pre; }
      const unsigned tlo = (unsigned)(tag >> 16) << 16, thi = (unsigned)(tag >> 32);
      if (r < 15 && lane < 40) {
        const unsigned eb = __builtin_bit_cast(unsigned, E);
        v4u o; o.x = tlo | (eb & 0xffffu); o.y = thi; o.z = tlo | (eb >> 16); o.w = thi;
        __builtin_amdgcn_raw_buffer_store_b128(o, rs, EG_OFF + (r * 1280 + 80 * j + cc) * 16, 0, AUX_SC1);
      }
      STAMP(2);
      if (lane < 40) {
#pragma unroll
        for (int s = 0; s < 16; ++s) es[s * 80 + cc] = ev[s];
      }
      float H = 0.f;
      if (r > 0) {
        float p5r, p5s;
        { const float a2r = lKr * lKr - lKi * lKi, a2i = 2.f * lKr * lKi, a4r = a2r * a2r - a2i * a2i, a4i = 2.f * a2r * a2i;
          p5r = a4r * lKr - a4i * lKi; const float p5i = a4r * lKi + a4i * lKr; p5s = (cc & 1) ? p5i : -p5i; }
        const int eo = EG_OFF + (80 * j + cc) * 16;
        v4u gv[15];
#pragma nounroll
        for (unsigned spins = 0; spins < SPIN_CAP; ++spins) {
#pragma unroll
          for (int m = 0; m < 15; ++m) gv[m] = ldsc1(rs, eo, (m < r ? r - 1 - m : 0) * (1280 * 16));
          unsigned bad = 0u, bad2 = 0u;
#pragma unroll
          for (int m = 0; m < 15; ++m) { bad |= (gv[m].x ^ tlo) | (gv[m].z ^ tlo); bad2 |= (gv[m].y ^ thi) | (gv[m].w ^ thi); }
          if (__all(((bad >> 16) | bad2) == 0u)) break;
          asm volatile("s_sleep 1" ::: "memory");
        }
        float hc[3] = {0.f, 0.f, 0.f};
#pragma unroll
        for (int m = 4; m >= 0; --m)
#pragma unroll
          for (int k = 0; k < 3; ++k) {
            const float Ev = (5 * k + m < r) ? __builtin_bit_cast(float, (gv[5 * k + m].x & 0xffffu) | (gv[5 * k + m].z << 16)) : 0.f;
            hc[k] = lKr * hc[k] + (lKs * swp1(hc[k]) + Ev);
          }
        H = p5r * hc[2] + (p5s * swp1(hc[2]) + hc[1]);
        H = p5r * H + (p5s * swp1(H) + hc[0]);
      }
      STAMP(3);
      if (lane < 40) ((LAS float*)(sm + HIN))[cc] = H;
      lds_signal(cnt + 1, lane);
    }
    {
      const f32x2 cr2 = f32x2{2.f * pm[240 + c], 2.f * pm[256 + c]}, ci2 = f32x2{2.f * pm[280 + c], 2.f * pm[296 + c]};
      const float cr4 = pm[272 + (c >> 1)], ca4 = (c & 1) ? -2.f * cr4 : 2.f * cr4, cb4 = -2.f * pm[312 + (c >> 1)];
      f32x2 gr = {1.f, 1.f}, gi = {0.f, 0.f}, g4r = {1.f, 1.f}, g4i = {0.f, 0.f};
      { f32x2 pr = {pm[80 + c], pm[96 + c]}, pi = {pm[120 + c], pm[136 + c]};
        f32x2 qr = {pm[112 + (c >> 1)], 0.f}, qi = {pm[152 + (c >> 1)], 0.f};
        const int sg = 4 * w + q;
#pragma unroll
        for (int bt = 0; bt < 4; ++bt) {
          const f32x2 tr_ = gr * pr - gi * pi, ti_ = gr * pi + gi * pr, ur_ = g4r * qr - g4i * qi, ui_ = g4r * qi + g4i * qr;
          if ((sg >> bt) & 1) { gr = tr_; gi = ti_; g4r = ur_; g4i = ui_; }
          if (bt < 3) { const f32x2 n1 = pr * pr - pi * pi, n2 = qr * qr - qi * qi; pi = 2.f * pr * pi; pr = n1; qi = 2.f * qr * qi; qr = n2; }
        } }
      const float g4s = (c & 1) ? g4i[0] : -g4i[0];
      lds_wait(cnt + 1, 2u);
      LAS unsigned char* scr = sm + w * 10240;
      LAS float* hin = (LAS float*)(sm + HIN);
      const f32x2 Hr = {hin[2 * c], hin[32 + 2 * c]}, Hi = {hin[2 * c + 1], hin[33 + 2 * c]};
      f32x2 hr = {es[(4 * w + q) * 80 + 2 * c], es[(4 * w + q) * 80 + 32 + 2 * c]}, hi = {es[(4 * w + q) * 80 + 2 * c + 1], es[(4 * w + q) * 80 + 33 + 2 * c]};
      hr += gr * Hr - gi * Hi; hi += gr * Hi + gi * Hr;
      float h4 = es[(4 * w + q) * 80 + 64 + c] + (g4r[0] * hin[64 + c] + g4s * hin[64 + (c ^ 1)]), hp4 = swp1(h4);
      f32x2 o2[2][8]; float o4[8];
#pragma unroll
      for (int s = 0; s < 8; ++s) {
        const f32x2 nr = lr2 * hr + (ur[s] - li2 * hi), ni = lr2 * hi + (li2 * hr + ui[s]);
        hr = nr; hi = ni;
        o2[0][s] = cr2 * hr - ci2 * hi;
        o2[1][s] = -(cr2 * hi + ci2 * hr);
        h4 = lr4 * h4 + (ls4 * hp4 + u4[s]); hp4 = swp1(h4);
        o4[s] = ca4 * h4 + cb4 * hp4;
      }
#pragma unroll
      for (int p = 0; p < 2; ++p)
#pragma unroll
        for (int pt = 0; pt < 2; ++pt) {
          v4u ov; ov.x = pk2(o2[pt][0][p], o2[pt][1][p]); ov.y = pk2(o2[pt][2][p], o2[pt][3][p]); ov.z = pk2(o2[pt][4][p], o2[pt][5][p]); ov.w = pk2(o2[pt][6][p], o2[pt][7][p]);
          *(LAS v4u*)(scr + (32 * p + 2 * c + pt) * 80 + q * 16) = ov;
        }
      { v4u ov; ov.x = pk2(o4[0], o4[1]); ov.y = pk2(o4[2], o4[3]); ov.z = pk2(o4[4], o4[5]); ov.w = pk2(o4[6], o4[7]);
        *(LAS v4u*)(scr + (64 + c) * 80 + q * 16) = ov; }
      WAITL0();
      const int qq = (lane & 15) >> 2, pp = lane & 3;
#pragma unroll
      for (int m = 0; m < 5; ++m) {
        const int unit = 4 * m + g, cch = unit >> 1, th = unit & 1;
        LAS unsigned char* ra = scr + (8 * cch + qq) * 80 + (16 * th + 4 * pp) * 2;
        const v2u lo = trread(ra), hi = trread(ra + 4 * 80);
        v4u ov; ov.x = lo.x; ov.y = lo.y; ov.z = hi.x; ov.w = hi.y;
        const int kap = 10 * j + cch;
        const int off = HT_OFF + (((2 * r + (w >> 1)) * 40 + (kap >> 2)) * 4 + 2 * (w & 1) + th) * 1024 + (kap & 3) * 256 + n16 * 16;
        __builtin_amdgcn_raw_buffer_store_b128(ov, rs, off, 0, AUX_SC1);
      }
    }
    STAMP(4);
    SCHED();
    waitv<0>();
    if (lane == 0) __hip_atomic_store((GAS u64*)(ws + FLAG_H_OFF) + ((r * 2 + (w >> 1)) * 16 + j) * 2 + (w & 1), tag, RLX_AGENT);
    STAMP(5);
#pragma unroll
    for (int k = PBY; k < DBY; ++k)
#pragma unroll
      for (int n = 0; n < 5; ++n) bw[k][n] = ldsc1(rs, boff + n * 1024, rotk<10>(k, s2) * 5120);
#pragma unroll
    for (int k = 2; k < 4; ++k)
#pragma unroll
      for (int n = 0; n < 5; ++n) dma1s(ws + rotk<10>(DBY + k, s2) * 5120 + n * 1024, boff, smb + w * 10240 + (k - 2) * 5120 + n * 1024);
    poll_flags((const GAS u64*)(ws + FLAG_H_OFF) + ((r * 2 + half2) * 16 + 4 * w) * 2 + (lane & 7), tag);
    STAMP(6);
    f32x4 ac2[5][4];
#pragma unroll
    for (int n = 0; n < 5; ++n)
#pragma unroll
      for (int i = 0; i < 4; ++i) ac2[n][i] = f32x4{0.f, 0.f, 0.f, 0.f};
    int aoff[4];
#pragma unroll
    for (int i = 0; i < 4; ++i) aoff[i] = HT_OFF + ((rt64 * 40 + 10 * w) * 4 + i) * 1024 + lane * 16;
    stream_gemm<10, DBY, DAY, NLY>(rs, boff, aoff, s2, bw, ac2,
        [&](int k) __attribute__((always_inline)) { return sm + (k < 2 ? yslot(w, k) : w * 10240 + (k - 2) * 5120) + lane * 16; }, [&]() __attribute__((always_inline)) {});
    STAMP(7);
    {
#pragma unroll
      for (int i = 0; i < 4; ++i)
#pragma unroll
        for (int n = 0; n < 5; ++n) *(LAS f32x4*)(sm + (i < 2 ? w * 10240 + i * 5120 : yslot(w, i - 2)) + n * 1024 + lane * 16) = ac2[n][i];
      lds_signal(cnt + 3, lane);
    }
    STAMP(8); STAMPC(23);
    STAMPS_OUT(); TS_OUT();
  } else {
    const int t32 = lane & 31, hh = lane >> 5;
    const int ctu = b & 7, fu = b >> 3;
    const int srow = 8 * j + 2 * w + hh;
    TP(1);
    if (w < 3) {
#pragma unroll
      for (int q3 = 0; q3 < 3; ++q3) { int ch = 64 * q3 + lane; if (ch > 159) ch = 159;
        dma1(w2, (unsigned)(48 * fu + 16 * w + ch / 10) * 1280u + ctu * 160 + (ch % 10) * 16, smb + W2SCR + w * 3072 + q3 * 1024); }
    } else {
      const unsigned o1 = (unsigned)(40 * r + lane / 12) * 3072u + j * 192 + (lane % 12) * 16;
      if (lane < 60) {
#pragma unroll
        for (int q8 = 0; q8 < 8; ++q8) dma1((const unsigned char*)w1 + q8 * (5 * 3072), o1, smb + W1SCR + q8 * 960);
      }
    }
    TP(2);
    v2u xr[5], br[5];
    { const unsigned char* xp = (const unsigned char*)x + (size_t)(128 * r + srow) * 1280 + t32 * 8;
#pragma unroll
      for (int k = 0; k < 5; ++k) xr[k] = *(const v2u*)(xp + k * 256); }
    const v2u ep = __builtin_amdgcn_raw_buffer_load_b64(rs, EPOCH_OFF, 0, 0);
    TP(3);
    int cbcol = 2 * w + hh; cbcol = 5 * r + (cbcol > 4 ? 4 : cbcol);
    { const int tt = cbcol >> 4, cc = cbcol & 15, part = tt < 4 ? (tt & 1) : (cc & 1), chn = tt < 4 ? 16 * (tt >> 1) + cc : 32 + (cc >> 1);
      const unsigned char* bp = (const unsigned char*)(part ? Bim : Bre) + (size_t)(40 * j + chn) * 1280 + t32 * 8;
#pragma unroll
      for (int k = 0; k < 5; ++k) br[k] = *(const v2u*)(bp + k * 256); }
    const unsigned dbv = *(const unsigned*)((hh ? b2 : Dsk) + 80 * ct + 2 * t32);
    const unsigned dbw = *(const unsigned*)((hh ? b2 : Dsk) + 80 * ct + 64 + 2 * (t32 & 7));
    const int pc = 40 * j + (lane < 40 ? lane : 39);
    const unsigned plr = lre[pc], pli = lim[pc], pdt = logdt[pc], b1v = *(const unsigned*)(b1 + 96 * j + 2 * (lane < 48 ? lane : 47));
    TP(4);
    const u64 tag = mix64(((u64)__builtin_amdgcn_readfirstlane(ep.y) << 32) | (unsigned)__builtin_amdgcn_readfirstlane(ep.x));
    {
      const unsigned ones = 0x3f803f80u;
      float a1 = 0.f;
#pragma unroll
      for (int k = 0; k < 5; ++k) { a1 = dot2(xr[k].x, ones, a1); a1 = dot2(xr[k].y, ones, a1); }
      a1 = sum32(a1);
      const float mean = a1 * (1.f / D);
      f32x2 q2 = {0.f, 0.f};
#pragma unroll
      for (int k = 0; k < 5; ++k) {
        f32x2 d0 = f32x2{bf2f(xr[k].x & 0xffffu), bfhi(xr[k].x)} - mean, d1 = f32x2{bf2f(xr[k].y & 0xffffu), bfhi(xr[k].y)} - mean;
        q2 += d0 * d0; q2 += d1 * d1;
      }
      float a2 = q2[0] + q2[1];
      a2 = sum32(a2);
      const f32x2 st = {__builtin_amdgcn_rcpf(rbf(__builtin_amdgcn_sqrtf(rbf(rbf(a2 * (1.f / D)) + 1e-5f)))), rbf(mean)};
      asm volatile("" :: "v"(br[0]), "v"(br[1]), "v"(br[2]), "v"(br[3]), "v"(br[4]), "v"(dbv), "v"(dbw), "v"(plr), "v"(pli), "v"(pdt), "v"(b1v) : "memory");
      TP(5);
      if (t32 == 0) __builtin_amdgcn_raw_buffer_store_b64(__builtin_bit_cast(v2u, st), rs, PRS_OFF + (r * 128 + srow) * 8, 0, AUX_SC1);
      TP(6);
      if (w < 3) {
        float c1 = 0.f;
#pragma unroll
        for (int k = 0; k < 5; ++k) { c1 = dot2(br[k].x, ones, c1); c1 = dot2(br[k].y, ones, c1); }
        c1 = sum32(c1);
        if (t32 == 0) __builtin_amdgcn_raw_buffer_store_b32(__builtin_bit_cast(unsigned, c1), rs, PCB_OFF + (j * 256 + cbcol) * 4, 0, AUX_SC1);
        if (w == 2) {
          *(LAS f32x2*)(sm + DB2 + hh * 320 + t32 * 8) = f32x2{bf2f(dbv & 0xffffu), bfhi(dbv)};
          *(LAS f32x2*)(sm + DB2 + hh * 320 + 256 + (t32 & 7) * 8) = f32x2{bf2f(dbw & 0xffffu), bfhi(dbw)};
        }
        const int gl = (lane >> 4) & 1, f16 = 3 * fu + w;
#pragma unroll
        for (int q3 = 0; q3 < 3; ++q3) {
          const int nt = q3 < 2 ? 2 * q3 + hh : 4;
          LAS unsigned char* ra = sm + W2SCR + w * 3072 + (4 * gl + ((lane & 15) >> 2)) * 160 + (16 * nt + 4 * (lane & 3)) * 2;
          const v2u lo = trread(ra), hi = trread(ra + 8 * 160);
          v4u ov; ov.x = lo.x; ov.y = lo.y; ov.z = hi.x; ov.w = hi.y;
          __builtin_amdgcn_raw_buffer_store_b128(ov, rs, WT_OFF + ((ctu * 88 + 40 + (f16 >> 1)) * 5 + nt) * 1024 + (2 * (f16 & 1) + gl) * 256 + n16 * 16, 0, AUX_SC1);
        }
      } else {
        const float dt = rbf(__expf(bf2f(pdt)));
        const float lr = bf2f(plr), li = bf2f(pli);
        const float mag = __expf(dt * lr);
        float rev = dt * li * 0.15915494309189535f; rev -= floorf(rev);
        const float ar = mag * __builtin_amdgcn_cosf(rev), ai = mag * __builtin_amdgcn_sinf(rev);
        const float nr = ar - 1.f, den = __builtin_amdgcn_rcpf(lr * lr + li * li);
        const float cr = (nr * lr + ai * li) * den, ci = (ai * lr - nr * li) * den;
        float pr = ar, pi = ai;
#pragma unroll
        for (int s = 0; s < 3; ++s) { const float t = pr * pr - pi * pi; pi = 2.f * pr * pi; pr = t; }
        const float p8r = pr, p8i = pi;
#pragma unroll
        for (int s = 0; s < 4; ++s) { const float t = pr * pr - pi * pi; pi = 2.f * pr * pi; pr = t; }
        if (lane < 40) {
          LAS float* pm = (LAS float*)(sm + PRM) + lane;
          pm[0] = ar; pm[40] = ai; pm[80] = p8r; pm[120] = p8i; pm[160] = pr; pm[200] = pi; pm[240] = cr; pm[280] = ci;
        }
        if (lane < 48) { f32x2 bb = {bf2f(b1v & 0xffffu), bfhi(b1v)}; *(LAS f32x2*)(sm + B1F + lane * 8) = bb; }
        if (lane == 0) *(LAS u64*)(sm + TAGL) = tag;
        float cs6[6] = {0.f, 0.f, 0.f, 0.f, 0.f, 0.f};
#pragma unroll
        for (int m = 0; m < 3; ++m) {
          const int kq = m < 2 ? g + 4 * m : 8 + (g & 1);
          const unsigned wgt = (m < 2 || g < 2) ? ones : 0u;
          LAS unsigned char* ra = sm + W1SCR + (4 * kq + ((lane & 15) >> 2)) * 192 + 8 * (lane & 3);
#pragma unroll
          for (int c6 = 0; c6 < 6; ++c6) { const v2u v = trread(ra + c6 * 32); cs6[c6] = dot2(v.y, wgt, dot2(v.x, wgt, cs6[c6])); }
        }
#pragma unroll
        for (int c6 = 0; c6 < 6; ++c6) cs6[c6] = add32(add16(cs6[c6]));
        const float v0 = g == 0 ? cs6[0] : g == 1 ? cs6[1] : g == 2 ? cs6[2] : cs6[3], v1 = (g & 1) ? cs6[5] : cs6[4];
        const int po = PCF_OFF + (j * 16 + r) * 384;
        __builtin_amdgcn_raw_buffer_store_b32(__builtin_bit_cast(unsigned, v0), rs, po + lane * 4, 0, AUX_SC1);
        __builtin_amdgcn_raw_buffer_store_b32(__builtin_bit_cast(unsigned, v1), rs, po + 256 + t32 * 4, 0, AUX_SC1);
      }
    }
    const int fx = (((t32 >> 3) & 3) << 1) | ((t32 >> 1) & 1);
    const int xa0 = XS + (32 * w + t32) * 128 + ((hh ^ fx) << 4);
    const int wa0 = W1S + (8 * hh + ((lane & 15) >> 2)) * 192 + (16 * ((lane >> 4) & 1) + 4 * (lane & 3)) * 2;
    f32x16 acc[3];
#pragma unroll
    for (int n = 0; n < 3; ++n)
#pragma unroll
      for (int e = 0; e < 16; ++e) acc[n][e] = 0.f;
    v4u xf[2][4]; v2u wf[2][4][3][2];
    auto rdF = [&](int set, int st, int q) __attribute__((always_inline)) {
      LAS unsigned char* sb = sm + (st % 3) * STAGE;
      const int s = q / 7, e = q % 7;
      if (e == 0) xf[set][s] = *(LAS v4u*)(sb + (xa0 ^ (s * 32)));
      else wf[set][s][(e - 1) >> 1][(e - 1) & 1] = trread(sb + wa0 + s * 3072 + ((e - 1) & 1) * 768 + ((e - 1) >> 1) * 64);
    };
    auto wfrag = [&](int set, int s, int n) __attribute__((always_inline)) { v4u v; v.x = wf[set][s][n][0].x; v.y = wf[set][s][n][0].y; v.z = wf[set][s][n][1].x; v.w = wf[set][s][n][1].y; return v; };
    auto mmF = [&](int set, int m) __attribute__((always_inline)) { const int s = m / 3, n = m % 3;
      acc[n] = mfma32(wfrag(set, s, n), xf[set][s], acc[n]);
    };
    WAITL0();
    TP(7);
    BAR();
    TP(8);
    STAMP(9);
#pragma unroll
    for (int q = 0; q < 28; ++q) rdF(0, 0, q);
    SCHED();
#pragma unroll
    for (int m = 0; m < 12; ++m) { mmF(0, m); SCHED(); }
    TS(1); TS(2); BAR(); TS(3);
#pragma unroll
    for (int q = 0; q < 28; ++q) rdF(1, 1, q);
    WAITL0(); BAR();
    v4u bw[DBF][5];
    const int boff = WT_OFF + (ct * 88 + 40 + 12 * w) * 5120 + lane * 16;
    const int s2 = s2f;
#pragma unroll
    for (int i = 1; i < 10; ++i) {
#pragma unroll
      for (int m = 0; m < 8; ++m) {
        mmF(i & 1, m);
        if (i < 9) {
#pragma unroll
          for (int q = (m < 4 ? 4 * m : 3 * m + 4); q < (m < 4 ? 4 * m + 4 : 3 * m + 7); ++q) rdF((i + 1) & 1, i + 1, q);
        }
        SCHED();
      }
      if (i == 8) { WAITL0(); BAR(); }
      if (i < 8) {
        WAITL0();
        TS(1 + 3 * i);
        if (i == 1) waitv<0>();
        TS(2 + 3 * i);
        BAR();
        TS(3 + 3 * i);
      }
      SCHED();
#pragma unroll
      for (int m = 8; m < 12; ++m) {
        mmF(i & 1, m);
        if (i >= 8) {
          bw[i - 8][m - 8] = ldsc1(rs, boff + (m - 8) * 1024, rotk<12>(i - 8, s2) * 5120);
          if (m == 11) bw[i - 8][4] = ldsc1(rs, boff + 4096, rotk<12>(i - 8, s2) * 5120);
        }
        SCHED();
      }
    }
    STAMP(1); TS(25);
    {
      const f32x2 st2 = *(LAS f32x2*)(sm + ST2 + (32 * w + t32) * 8);
      const float rsd = st2[0], nm = -rsd * st2[1];
      const int hoff = H1T_OFF + (((2 * r + (w >> 1)) * 48 + 3 * j) * 4 + 2 * (w & 1) + (t32 >> 4)) * 1024 + hh * 256 + (t32 & 15) * 16;
#pragma unroll
      for (int n = 0; n < 3; ++n)
#pragma unroll
        for (int hf = 0; hf < 2; ++hf) {
          unsigned ou[4];
#pragma unroll
          for (int rq = 0; rq < 2; ++rq) {
            const int nb = 32 * n + 8 * (2 * hf + rq) + 4 * hh;
            const f32x4 S = *(LAS f32x4*)(sm + CSF + nb * 4), bb = *(LAS f32x4*)(sm + B1F + nb * 4);
#pragma unroll
            for (int e = 0; e < 2; ++e) {
              const f32x2 a = {acc[n][8 * hf + 4 * rq + 2 * e], acc[n][8 * hf + 4 * rq + 2 * e + 1]};
              const f32x2 S2 = {S[2 * e], S[2 * e + 1]}, b2_ = {bb[2 * e], bb[2 * e + 1]};
              const f32x2 v = rsd * a + (nm * S2 + b2_);
              const f32x2 z = v * (-2.302208198f - 0.1029432397f * (v * v));
              f32x2 d = {__builtin_amdgcn_exp2f(z[0]), __builtin_amdgcn_exp2f(z[1])};
              d += 1.f;
              const f32x2 rc = {__builtin_amdgcn_rcpf(d[0]), __builtin_amdgcn_rcpf(d[1])};
              const f32x2 gl = v * rc;
              ou[2 * rq + e] = pk2(gl[0], gl[1]);
            }
          }
          v4u ov; ov.x = ou[0]; ov.y = ou[1]; ov.z = ou[2]; ov.w = ou[3];
          __builtin_amdgcn_raw_buffer_store_b128(ov, rs, hoff + n * 4096 + hf * 512, 0, AUX_SC1);
          SCHED();
        }
    }
    STAMP(4);
    waitv<0>();
    if (lane == 0) __hip_atomic_store((GAS u64*)(ws + FLAG_F_OFF) + ((r * 2 + (w >> 1)) * 16 + j) * 2 + (w & 1), tag, RLX_AGENT);
    STAMP(5);
#pragma unroll
    for (int k = PBF; k < DBF; ++k)
#pragma unroll
      for (int n = 0; n < 5; ++n) bw[k][n] = ldsc1(rs, boff + n * 1024, rotk<12>(k, s2) * 5120);
    poll_flags((const GAS u64*)(ws + FLAG_F_OFF) + ((r * 2 + half2) * 16 + 4 * w) * 2 + (lane & 7), tag);
    STAMP(6);
    f32x4 ac2[5][4];
#pragma unroll
    for (int n = 0; n < 5; ++n)
#pragma unroll
      for (int i = 0; i < 4; ++i) ac2[n][i] = f32x4{0.f, 0.f, 0.f, 0.f};
    int aoff[4];
#pragma unroll
    for (int i = 0; i < 4; ++i) aoff[i] = H1T_OFF + ((rt64 * 48 + 12 * w) * 4 + ((i + w) & 3)) * 1024 + lane * 16;
    const int trow = 64 * half2 + 16 * w + n16;
    const size_t orow = (size_t)(128 * r + trow) * D + 80 * ct + 4 * g;
    v2u xv[5];
    stream_gemm<12, DBF, DAF, NLF>(rs, boff, aoff, s2, bw, ac2,
        [&](int k) __attribute__((always_inline)) { if (k == 0) lds_wait(cnt + 4 + w, 1u); return sm + fslot(w, k) + lane * 16; },
        [&]() __attribute__((always_inline)) {
#pragma unroll
          for (int n = 0; n < 5; ++n) xv[n] = *(const v2u*)(x + orow + 16 * n);
        });
    STAMP(7);
    {
#pragma unroll
      for (int i = 1; i < 4; ++i)
#pragma unroll
        for (int n = 0; n < 5; ++n) *(LAS f32x4*)(sm + fslot(w, i - 1) + n * 1024 + lane * 16) = ac2[n][i];
      lds_signal(cnt + 2, lane);
    }
    const float ers = ((LAS float*)(sm + ST2))[2 * trow], emb = ((LAS float*)(sm + ST2))[2 * trow + 1];
    lds_wait(cnt + 2, 4u);
    f32x4 gg[5], sk[5], xx[5];
#pragma unroll
    for (int n = 0; n < 5; ++n) {
      f32x4 h2 = ac2[n][0];
#pragma unroll
      for (int o = 1; o < 4; ++o) {
        const int wp = (w + o) & 3;
        h2 += *(LAS f32x4*)(sm + fslot(wp, 3 - o) + n * 1024 + lane * 16);
      }
      const float xs[4] = {bf2f(xv[n].x & 0xffffu), bfhi(xv[n].x), bf2f(xv[n].y & 0xffffu), bfhi(xv[n].y)};
      const f32x4 ds = *(LAS f32x4*)(sm + DB2 + (16 * n + 4 * g) * 4), bs = *(LAS f32x4*)(sm + DB2 + 320 + (16 * n + 4 * g) * 4);
#pragma unroll
      for (int e = 0; e < 4; ++e) {
        const float a = rbf(rbf(h2[e]) + bs[e]);
        gg[n][e] = rbf(a * __builtin_amdgcn_rcpf(1.f + __builtin_amdgcn_exp2f(-1.4426950408889634f * a)));
        const float xn = rbf(rbf(xs[e] - emb) * ers);
        sk[n][e] = rbf(xn * ds[e]);
        xx[n][e] = xs[e];
      }
    }
    STAMP(2);
    lds_wait(cnt + 3, 4u);
    STAMP(3);
#pragma unroll
    for (int n = 0; n < 5; ++n) {
      f32x4 y = sk[n];
#pragma unroll
      for (int o = 0; o < 4; ++o) y += *(LAS f32x4*)(sm + (w < 2 ? o * 10240 + w * 5120 : yslot(o, w - 2)) + n * 1024 + lane * 16);
      *(f32x4*)(out + orow + 16 * n) = xx[n] + y * gg[n];
    }
    STAMP(8); STAMPC(23);
    STAMPS_OUT(); TS_OUT();
  }
}
}

extern "C" void kernel_launch(void* const* d_in, const int* in_sizes, int n_in,
                              void* d_out, int out_size, void* d_ws, size_t ws_size,
                              hipStream_t stream) {
  using s5k::u16;
  (void)hipFuncSetAttribute((const void*)s5k::s5_fused, hipFuncAttributeMaxDynamicSharedMemorySize, s5k::LDS_BYTES);
  s5k::s5_fused<<<s5k::NWG, s5k::NTHR, s5k::LDS_BYTES, stream>>>(
      (const u16*)d_in[0], (const u16*)d_in[1], (const u16*)d_in[2], (const u16*)d_in[3], (const u16*)d_in[4], (const u16*)d_in[5],
      (const u16*)d_in[6], (const u16*)d_in[7], (const u16*)d_in[8], (const u16*)d_in[10], (const u16*)d_in[11], (const u16*)d_in[12],
      (const u16*)d_in[13], (float*)d_out, (unsigned char*)d_ws);
}
```

```cpp
#include <hip/hip_runtime.h>
#include <hip/hip_bf16.h>
#include <stdint.h>
#include <type_traits>

namespace s5k {
typedef unsigned short u16;
typedef unsigned long long u64;
typedef unsigned v4u __attribute__((ext_vector_type(4)));
typedef unsigned v2u __attribute__((ext_vector_type(2)));
typedef float f32x4 __attribute__((ext_vector_type(4)));
typedef float f32x2 __attribute__((ext_vector_type(2)));
typedef float f32x16 __attribute__((ext_vector_type(16)));
typedef __bf16 bf16x8 __attribute__((ext_vector_type(8)));
typedef __bf16 bf16x2 __attribute__((ext_vector_type(2)));
typedef short s16x4 __attribute__((ext_vector_type(4)));
#define GAS __attribute__((address_space(1)))
#define LAS __attribute__((address_space(3)))
#define RLX_AGENT __ATOMIC_RELAXED, __HIP_MEMORY_SCOPE_AGENT
#define RLX_WG __ATOMIC_RELAXED, __HIP_MEMORY_SCOPE_WORKGROUP

constexpr int L = 2048, D = 640, P = 640, F = 1536;
constexpr int NWG = 256, NTHR = 512;
constexpr int EPOCH_OFF = 0;
constexpr int FLAG_E_OFF = 4096;
constexpr int FLAG_F_OFF = 65536;
constexpr int FLAG_H_OFF = 65536 + 8192;
constexpr int EG_OFF = 131072;
constexpr int PRS_OFF = 512 << 10;
constexpr int PCB_OFF = 576 << 10;
constexpr int PCF_OFF = 640 << 10;
constexpr int WT_OFF = 1 << 20;
constexpr int H1T_OFF = 8 << 20;
constexpr int HT_OFF = 16 << 20;
constexpr int WS_BYTES = 24 << 20;
constexpr int XS = 0, BWS = 16384, W1S = 26624, STAGE = 38912;
constexpr int XR = 3 * STAGE;
constexpr int XROW = XR, BROW = XR + 10240, W1SCR = BROW + 6400, W2SCR = W1SCR + 7680, CSCR = W2SCR + 7680, RAW = CSCR + 8192;
constexpr int MISC = 158976;
static_assert(RAW + 1024 <= MISC, "");
constexpr int ST2 = MISC;
constexpr int CSB = MISC + 1024;
constexpr int CSF = MISC + 2048;
constexpr int B1F = MISC + 2432;
constexpr int PRM = MISC + 2816;
constexpr int CNT = MISC + 4096;
constexpr int DB2 = MISC + 4160;
constexpr int HIN = CSB + 384;
constexpr int LDS_BYTES = MISC + 4864;
static_assert(HIN + 640 <= LDS_BYTES, "");
constexpr int SLOT0 = 40960;
constexpr int ESR = SLOT0 + 20 * 5120;
__device__ __forceinline__ int yslot(int w, int k) { return SLOT0 + (k ? 11 + w : (w == 3 ? 19 : 4 + w)) * 5120; }
__device__ __forceinline__ int fslot(int w, int k) { return SLOT0 + (k == 0 ? 15 + w : k == 1 ? w : 7 + w) * 5120; }
constexpr int SKR = ESR + 5120;
constexpr int JUNK = SKR + 10240;
static_assert(JUNK + 256 <= MISC, "");
static_assert(LDS_BYTES <= 163840, "");
constexpr unsigned SPIN_CAP = 1u << 20;
constexpr int AUX_SC1 = 16;

__device__ __forceinline__ float bf2f(unsigned b) { return __builtin_bit_cast(float, b << 16); }
__device__ __forceinline__ float bfhi(unsigned b) { return __builtin_bit_cast(float, b & 0xffff0000u); }
__device__ __forceinline__ unsigned pk2(float a, float b) { f32x2 f = {a, b}; return __builtin_bit_cast(unsigned, __builtin_convertvector(f, bf16x2)); }
__device__ __forceinline__ float rbf(float a) { return bf2f(pk2(a, a) & 0xffffu); }
__device__ __forceinline__ float dot2(unsigned a, unsigned b, float c) {
  return __builtin_amdgcn_fdot2_f32_bf16(__builtin_bit_cast(bf16x2, a), __builtin_bit_cast(bf16x2, b), c, false);
}
__device__ __forceinline__ float sum8(v4u a, float c) {
  const unsigned ones = 0x3f803f80u;
  c = dot2(a.x, ones, c); c = dot2(a.y, ones, c); c = dot2(a.z, ones, c); c = dot2(a.w, ones, c); return c;
}
__device__ __forceinline__ float sq8(v4u a, float c) {
  c = dot2(a.x, a.x, c); c = dot2(a.y, a.y, c); c = dot2(a.z, a.z, c); c = dot2(a.w, a.w, c); return c;
}
__device__ __forceinline__ float swp1(float v) {
  return __builtin_bit_cast(float, __builtin_amdgcn_update_dpp(0, __builtin_bit_cast(int, v), 0xB1, 0xF, 0xF, true));
}
template <int CTRL> __device__ __forceinline__ float dppf(float v) {
  return __builtin_bit_cast(float, __builtin_amdgcn_update_dpp(0, __builtin_bit_cast(int, v), CTRL, 0xF, 0xF, true));
}
__device__ __forceinline__ float add16(float v) {
  float c_ = v;
  asm("s_nop 1\n\tv_permlane16_swap_b32 %0, %1" : "+v"(v), "+v"(c_));
  return v + c_;
}
__device__ __forceinline__ float add32(float v) {
  float c_ = v;
  asm("s_nop 1\n\tv_permlane32_swap_b32 %0, %1" : "+v"(v), "+v"(c_));
  return v + c_;
}
__device__ __forceinline__ float sum32(float v) {
  v += dppf<0xB1>(v); v += dppf<0x4E>(v); v += dppf<0x141>(v); v += dppf<0x140>(v); return add16(v);
}
__device__ __forceinline__ f32x2 cmad(f32x2 lrr, f32x2 lsi, f32x2 h, f32x2 u) {
  return lrr * h + (lsi * __builtin_shufflevector(h, h, 1, 0) + u);
}
__device__ __forceinline__ f32x4 mfma16(v4u a, v4u b, f32x4 c) {
  return __builtin_amdgcn_mfma_f32_16x16x32_bf16(__builtin_bit_cast(bf16x8, a), __builtin_bit_cast(bf16x8, b), c, 0, 0, 0);
}
__device__ __forceinline__ f32x16 mfma32(v4u a, v4u b, f32x16 c) {
  return __builtin_amdgcn_mfma_f32_32x32x16_bf16(__builtin_bit_cast(bf16x8, a), __builtin_bit_cast(bf16x8, b), c, 0, 0, 0);
}
__device__ __forceinline__ v2u trread(LAS unsigned char* p) {
  return __builtin_bit_cast(v2u, __builtin_amdgcn_ds_read_tr16_b64_v4i16((LAS s16x4*)p));
}
__device__ __forceinline__ void dma1(const void* gb, unsigned vo, unsigned ld) {
  asm volatile("s_mov_b32 m0, %2\n\ts_nop 0\n\tglobal_load_lds_dwordx4 %1, %0" :: "s"(gb), "v"(vo), "s"(ld) : "memory");
}
__device__ __forceinline__ void dma1h(const void* gb, unsigned vo, unsigned ld) {
  asm volatile("s_mov_b32 m0, %2\n\ts_mov_b32 exec_hi, 0\n\tglobal_load_lds_dwordx4 %1, %0\n\ts_mov_b32 exec_hi, -1" :: "s"(gb), "v"(vo), "s"(ld) : "memory");
}
__device__ __forceinline__ void dma1s(const void* gb, unsigned vo, unsigned ld) {
  asm volatile("s_mov_b32 m0, %2\n\ts_nop 0\n\tglobal_load_lds_dwordx4 %1, %0 sc1" :: "s"(gb), "v"(vo), "s"(ld) : "memory");
}
template <int N> __device__ __forceinline__ void waitv() { asm volatile("s_waitcnt vmcnt(%0)" :: "i"(N) : "memory"); }
#define SCHED() __builtin_amdgcn_sched_barrier(0)
#define ABL_ST(x) x
#define WAITL0() asm volatile("s_waitcnt lgkmcnt(0)" ::: "memory")
#define BAR() __builtin_amdgcn_s_barrier()
#define LDU16(dst, ptr) asm volatile("global_load_ushort %0, %1, off" : "=v"(dst) : "v"(ptr) : "memory")
#define LDU32(dst, ptr) asm volatile("global_load_dword %0, %1, off" : "=v"(dst) : "v"(ptr) : "memory")
#define LDU64(dst, ptr) asm volatile("global_load_dwordx2 %0, %1, off" : "=v"(dst) : "v"(ptr) : "memory")
#define LDU128(dst, ptr) asm volatile("global_load_dwordx4 %0, %1, off" : "=v"(dst) : "v"(ptr) : "memory")

#define STAMP(k) do {} while (0)
#define TS(k) do {} while (0)
#define TQ(k) do {} while (0)
#define TG(k) do {} while (0)
#define TS_OUT() do {} while (0)

__device__ __forceinline__ u64 mix64(u64 z) {
  z += 0x9E3779B97F4A7C15ull; z = (z ^ (z >> 30)) * 0xBF58476D1CE4E5B9ull; z = (z ^ (z >> 27)) * 0x94D049BB133111EBull; return z ^ (z >> 31);
}
__device__ __forceinline__ void poll_flags(const GAS u64* f, u64 tag) {
#pragma nounroll
  for (unsigned spins = 0; spins < SPIN_CAP; ++spins) {
    const u64 v = __hip_atomic_load(f, RLX_AGENT);
    if (__all(v == tag)) break;
    __builtin_amdgcn_s_sleep(1);
  }
  __builtin_amdgcn_fence(__ATOMIC_ACQUIRE, "wavefront");
}
__device__ __forceinline__ void lds_signal(LAS unsigned* c, int lane) {
  WAITL0();
  if (lane == 0) __hip_atomic_fetch_add(c, 1u, RLX_WG);
}
__device__ __forceinline__ void lds_wait(LAS unsigned* c, unsigned want) {
#pragma nounroll
  for (unsigned spins = 0; spins < SPIN_CAP * 16; ++spins) {
    if (__hip_atomic_load(c, RLX_WG) >= want) break;
    __builtin_amdgcn_s_sleep(1);
  }
  asm volatile("" ::: "memory");
}
template <int NK> __device__ __forceinline__ int rotk(int k, int s) { const int a = k + s; return a >= NK ? a - NK : a; }
__device__ __forceinline__ v4u ldsc1(__amdgpu_buffer_rsrc_t rs, int voff, int soff) { return __builtin_amdgcn_raw_buffer_load_b128(rs, voff, soff, AUX_SC1); }

template <int NK, int DB, int DA, int NL, class LW, class HK, class HK2>
__device__ __forceinline__ void stream_gemm(__amdgpu_buffer_rsrc_t rs, int boff, const int (&aoff)[4], int s2, v4u (&bw)[DB][5], f32x4 (&acc)[5][4], LW lw, HK hook, HK2 hk2) {
  v4u aw[DA][4];
#pragma unroll
  for (int k = 0; k < DA; ++k) {
#pragma unroll
    for (int i = 0; i < 4; ++i) aw[k][i] = ldsc1(rs, aoff[i], rotk<NK>(k, s2) * 4096);
    SCHED();
  }
#pragma unroll
  for (int k = 0; k < NK; ++k) {
#pragma unroll
    for (int i = 0; i < 4; ++i) {
#pragma unroll
      for (int n = 0; n < 5; ++n) acc[n][i] = mfma16(bw[k % DB][n], aw[k % DA][i], acc[n][i]);
      SCHED();
      if (k + DA < NK) aw[k % DA][i] = ldsc1(rs, aoff[i], rotk<NK>(k + DA, s2) * 4096);
      SCHED();
    }
    if (k == NK - DA) hook();
    hk2(k);
    if (k + DB < NK) {
      if (k < NL) {
        LAS unsigned char* lp = lw(k);
#pragma unroll
        for (int n = 0; n < 5; ++n) bw[k % DB][n] = *(LAS v4u*)(lp + n * 1024);
      } else {
#pragma unroll
        for (int n = 0; n < 5; ++n) bw[k % DB][n] = ldsc1(rs, boff + n * 1024, rotk<NK>(k + DB, s2) * 5120);
      }
    }
    SCHED();
  }
}

#define ROT1 2
#define ROT2 0
#define DAF_ 3
#define DAY_ 3
constexpr int DBF = 5, DAF = DAF_, DBY = 5, DAY = DAY_, NLF = 3, NLY = 2;
constexpr int PBF = 2, PBY = 3;
__device__ __forceinline__ void dma1m(const void* gb, unsigned vo, unsigned ld, int sh) {
  asm volatile("s_mov_b32 m0, %2\n\ts_lshr_b64 exec, exec, %3\n\tglobal_load_lds_dwordx4 %1, %0\n\ts_mov_b64 exec, -1" :: "s"(gb), "v"(vo), "s"(ld), "s"(sh) : "memory", "scc");
}
#define IC(n) std::integral_constant<int, n>{}

#if defined(__HIP_DEVICE_COMPILE__) && !defined(USE_PK)
#define NOPK_ATTR __attribute__((target("no-packed-fp32-ops")))
#else
#define NOPK_ATTR
#endif
__global__ __launch_bounds__(NTHR, 2) NOPK_ATTR void s5_fused(const u16* __restrict__ x, const u16* __restrict__ lre, const u16* __restrict__ lim,
    const u16* __restrict__ Bre, const u16* __restrict__ Bim, const u16* __restrict__ Cre, const u16* __restrict__ Cim,
    const u16* __restrict__ logdt, const u16* __restrict__ Dsk, const u16* __restrict__ w1, const u16* __restrict__ b1,
    const u16* __restrict__ w2, const u16* __restrict__ b2, float* __restrict__ out, unsigned char* __restrict__ ws) {
  extern __shared__ __attribute__((aligned(16))) unsigned char smem_[];
  LAS unsigned char* sm = (LAS unsigned char*)smem_;
  asm volatile("" :: "s"(x), "s"(lre), "s"(lim), "s"(Bre), "s"(Bim), "s"(Cre), "s"(Cim), "s"(logdt), "s"(Dsk), "s"(w1), "s"(b1), "s"(w2), "s"(b2), "s"(out), "s"(ws));
  const int tid = threadIdx.x, lane = tid & 63;
  const int wv = __builtin_amdgcn_readfirstlane(tid >> 6), w = wv & 3;
  const int b = blockIdx.x, xcd = b & 7, idx = b >> 3;
  const int r = 4 * (xcd >> 1) + (idx & 3), jj = idx >> 2, j = 8 * (xcd & 1) + jj;
  const int half2 = jj & 1, ct = 4 * (xcd & 1) + (jj >> 1), rt64 = 2 * r + half2;
  const int s1 = ROT1 == 1 ? (j + 3 * r) % 10 : ROT1 == 2 ? 5 * ((j + r) & 1) : ROT1 == 3 ? 3 * ((j + r) % 3) : 0;
  STAMP(0); TS(0); TG(0);
  const unsigned smb = (unsigned)(size_t)sm;
  const __amdgpu_buffer_rsrc_t rs = __builtin_amdgcn_make_buffer_rsrc((void*)ws, 0, WS_BYTES, 0x00020000);
  LAS unsigned* cnt = (LAS unsigned*)(sm + CNT);
  const int n16 = lane & 15, g = lane >> 4;
  const int s2f = ROT2 ? 3 * (ct & 3) : 0, s2y = ROT2 ? (5 * (ct & 3)) >> 1 : 0;
  const int ctu = b & 7, fu = b >> 3;
  auto read_tag = [&]() __attribute__((always_inline)) {
    const v2u e = *(LAS v2u*)(sm + RAW + 47 * 16);
    return mix64(((u64)__builtin_amdgcn_readfirstlane(e.y) << 32) | (unsigned)__builtin_amdgcn_readfirstlane(e.x));
  };

  if (wv < 4) {
    unsigned so[10];
    { const int xrow = 32 * w + (lane >> 3);
      const unsigned xb = (unsigned)(128 * r + xrow) * 1280u + (((lane & 7) ^ ((xrow >> 1) & 1)) << 4);
#pragma unroll
      for (int q = 0; q < 4; ++q) so[q] = (xb + q * 10240u) ^ (q << 5); }
    const unsigned char* bmat = (const unsigned char*)((w >> 1) ? Bim : Bre);
    const unsigned l16 = lane * 16;
    auto job = [&](int d) __attribute__((always_inline)) {
      if (d < 100) {
        const int st = d / 10, p = d % 10, kt = rotk<10>(st, s1);
        const unsigned sb = smb + (st % 3) * STAGE;
        if (p < 4) dma1((const unsigned char*)x + kt * 128, so[p], sb + XS + (32 * w + 8 * p) * 128);
        else if (p < 6) dma1(bmat + kt * 128, so[p], sb + BWS + 2560 * w + (p - 4) * 1024);
        else if (p == 6) dma1h(bmat + kt * 128, so[p], sb + BWS + 2560 * w + 2048);
        else dma1((const unsigned char*)w1 + (size_t)kt * (64 * 3072), so[p], sb + W1S + 3072 * w + (p - 7) * 1024);
      } else if (d < 102) {
        const int pc = 2 * w + (d - 100);
        dma1s(ws + (pc == 0 ? PRS_OFF + r * 1024 : pc == 1 ? PCB_OFF + j * 1024 : PCF_OFF + j * 6144 + (pc - 2) * 1024), l16,
              smb + (pc == 0 ? ST2 : pc == 1 ? CSB : ESR + (pc - 2) * 1024));
      } else if (d < 127) {
        const int sl = (d - 102) / 5, n = (d - 102) % 5;
        if (sl == 2 || sl == 4) dma1s(ws + WT_OFF + (ct * 88 + 10 * w + rotk<10>(DBY + (sl >> 2), s2y)) * 5120 + n * 1024, l16, smb + yslot(w, sl >> 2) + n * 1024);
        else { const int k = sl == 3 ? 2 : sl; dma1s(ws + WT_OFF + (ct * 88 + 40 + 12 * w + rotk<12>(DBF + k, s2f)) * 5120 + n * 1024, l16, smb + fslot(w, k) + n * 1024); }
      }
    };
#pragma unroll
    for (int d = 0; d < 4; ++d) job(d);
#pragma unroll
    for (int q3 = 0; q3 < 3; ++q3) {
      int ch = 64 * q3 + lane; if (ch > 159) ch = 159;
      const int cl = 20 * (w & 1) + (ch >> 3), cs_ = ch & 7;
      so[4 + q3] = (unsigned)(40 * j + cl) * 1280u + ((cs_ ^ ((cl >> 1) & 7) ^ ((w >> 1) << 2)) << 4);
      const int c2 = 192 * w + 64 * q3 + lane;
      so[7 + q3] = (unsigned)(c2 / 12) * 3072u + j * 192 + (c2 % 12) * 16;
    }
#pragma unroll
    for (int d = 4; d < 10; ++d) job(d);
    if (tid < 16) cnt[tid] = 0u;
    const unsigned o1 = (unsigned)(40 * r + lane / 12) * 3072u + j * 192 + (lane % 12) * 16;
    const unsigned o2 = (unsigned)(48 * fu + 12 * w + lane / 10) * 1280u + ctu * 160 + (lane % 10) * 16;
    const int u = b * 4 + w, cu = u < 800 ? u : u - 800, cd8 = cu / 10, ccb = cu % 10;
    const unsigned o3 = (unsigned)((8 * cd8 + (lane >> 3)) * P + 64 * ccb + 8 * (lane & 7)) * 2;
    auto jobG2 = [&](int q) __attribute__((always_inline)) {
      const int p = 7 * w + q;
      if (p < 25) {
        const unsigned char* gb = p < 10 ? (const unsigned char*)x + (size_t)(128 * r + 8 * j) * 1280 + p * 1024
                                : p < 17 ? (const unsigned char*)(r < 8 ? Bre : Bim) + (size_t)(40 * j + 5 * (r & 7)) * 1280 + (p - 10) * 1024
                                         : (const unsigned char*)w1 + (p - 17) * (5 * 3072);
        dma1m(gb, p < 17 ? l16 : o1, smb + (p < 17 ? XROW + p * 1024 : W1SCR + (p - 17) * 960), p < 16 ? 0 : p == 16 ? 48 : 4);
      } else if (p == 25) {
        const u16* vb = lane < 5 ? lre + 40 * j + 8 * lane : lane < 10 ? lim + 40 * j + 8 * (lane - 5) : lane < 15 ? logdt + 40 * j + 8 * (lane - 10)
                      : lane < 27 ? b1 + 96 * j + 8 * (lane - 15) : lane < 37 ? Dsk + 80 * ct + 8 * (lane - 27) : lane < 47 ? b2 + 80 * ct + 8 * (lane - 37) : (const u16*)(ws + EPOCH_OFF);
        asm volatile("s_mov_b32 m0, %1\n\ts_nop 0\n\tglobal_load_lds_dwordx4 %0, off" :: "v"(vb), "s"(smb + RAW) : "memory");
      }
    };
    auto jobG1 = [&](int q) __attribute__((always_inline)) {
      if (q < 2) dma1m((const unsigned char*)w2 + q * (6 * 1280), o2, smb + W2SCR + (2 * w + q) * 960, 4);
      else dma1(q == 2 ? Cre : Cim, o3, smb + CSCR + w * 2048 + (q - 2) * 1024);
    };
    const int rho = n16;
    const int Rrow = 32 * w + 8 * (rho >> 2) + (rho & 3);
    const int fx = ((rho >> 2) << 1) | ((rho >> 1) & 1);
    const int xa0 = XS + Rrow * 128 + ((g ^ fx) << 4);
    const int wb0 = BWS + rho * 128 + ((g ^ ((rho >> 1) & 7)) << 4);
    const int wb4 = BWS + (40 * (rho & 1) + 32 + (rho >> 1)) * 128 + ((g ^ (rho >> 2) ^ ((rho & 1) << 2)) << 4);
    f32x4 acc[2][5];
#pragma unroll
    for (int i = 0; i < 2; ++i)
#pragma unroll
      for (int t = 0; t < 5; ++t) acc[i][t] = f32x4{0.f, 0.f, 0.f, 0.f};
    v4u fa[2][2][2], fb[2][2][5];
    auto rdB = [&](int set, int st, int q) __attribute__((always_inline)) {
      LAS unsigned char* sb = sm + (st % 3) * STAGE;
      const int kk = q / 7, e = q % 7;
      if (e < 2) fa[set][kk][e] = *(LAS v4u*)(sb + (xa0 ^ (kk * 64)) + e * 512);
      else if (e < 6) { const int t = e - 2; fb[set][kk][t] = *(LAS v4u*)(sb + (wb0 ^ ((kk ^ (t & 1)) * 64)) + (t & 1) * 5120 + (t >> 1) * 2048); }
      else fb[set][kk][4] = *(LAS v4u*)(sb + (wb4 ^ (kk * 64)));
    };
    auto mmB = [&](int set, int m) __attribute__((always_inline)) {
      const int kk = m / 10, t = (m % 10) >> 1, i = m & 1;
      acc[i][t] = mfma16(fa[set][kk][i], fb[set][kk][t], acc[i][t]);
    };
    jobG2(0); jobG2(1);
    waitv<2>();
    BAR();
    STAMP(9);
#pragma unroll
    for (int q = 2; q < 7; ++q) jobG2(q);
#pragma unroll
    for (int q = 0; q < 4; ++q) jobG1(q);
#pragma unroll
    for (int q = 0; q < 14; ++q) rdB(0, 0, q);
    SCHED();
#pragma unroll
    for (int m = 0; m < 20; ++m) { mmB(0, m); if (m & 1) job(10 + (m >> 1)); SCHED(); }
    waitv<10>(); BAR();
#pragma unroll
    for (int d = 20; d < 30; ++d) job(d);
    {
      LAS unsigned char* sc = sm + CSCR + w * 2048 + (lane & 7) * 128 + (lane >> 3) * 8;
      const v2u r0 = *(LAS v2u*)sc, i0 = *(LAS v2u*)(sc + 1024), r1 = *(LAS v2u*)(sc + 64), i1 = *(LAS v2u*)(sc + 1088);
      v4u o0, o1_;
      o0.x = __builtin_amdgcn_perm(i0.x, r0.x, 0x05040100u); o0.y = __builtin_amdgcn_perm(i0.x, r0.x, 0x07060302u);
      o0.z = __builtin_amdgcn_perm(i0.y, r0.y, 0x05040100u); o0.w = __builtin_amdgcn_perm(i0.y, r0.y, 0x07060302u);
      o1_.x = __builtin_amdgcn_perm(i1.x, r1.x, 0x05040100u); o1_.y = __builtin_amdgcn_perm(i1.x, r1.x, 0x07060302u);
      o1_.z = __builtin_amdgcn_perm(i1.y, r1.y, 0x05040100u); o1_.w = __builtin_amdgcn_perm(i1.y, r1.y, 0x07060302u);
      const int ctc = cd8 / 10, ntc = (cd8 % 10) >> 1;
      const int fo = WT_OFF + ((ctc * 88 + 4 * ccb + (lane >> 5)) * 5 + ntc) * 1024 + ((lane >> 3) & 3) * 256 + (8 * (cd8 & 1) + (lane & 7)) * 16;
      __builtin_amdgcn_raw_buffer_store_b128(o0, rs, fo, 0, AUX_SC1);
      __builtin_amdgcn_raw_buffer_store_b128(o1_, rs, fo + 2 * 5120, 0, AUX_SC1);
      SCHED();
    }
#pragma unroll
    for (int d = 30; d < 40; ++d) job(d);
    TS(1);
    waitv<22>(); TS(2); BAR(); TS(3);
#pragma unroll
    for (int q = 0; q < 14; ++q) rdB(1, 1, q);
    WAITL0(); waitv<12>(); BAR();
#pragma unroll
    for (int d = 40; d < 44; ++d) job(d);
    const u64 tag = read_tag();
    v4u efv[2];
    const int boff = WT_OFF + (ct * 88 + 10 * w) * 5120 + lane * 16;
    const int s2 = s2y;
    v4u bw[DBY][5];
    const int q = g, c = n16;
    LAS float* pm = (LAS float*)(sm + PRM);
    f32x4 st0, st1, st2_, st3; float Sc[5]; f32x2 lr2, li2; float lr4, ls4;
    auto stepB = [&](auto I_) __attribute__((always_inline)) {
      constexpr int i = decltype(I_)::value;
      auto jobA = [&](int a) __attribute__((always_inline)) {
        if (i <= 6) { if (a < 6) job(10 * (i + 3) + 4 + a); }
        else if (i == 7) job(106 + a);
        else if (i == 8) job(119 + a);
        else if (a < 5) bw[1][a] = ldsc1(rs, boff + a * 1024, rotk<10>(1, s2) * 5120);
      };
      auto jobB = [&](int a) __attribute__((always_inline)) {
        if (i <= 5) { if (a < 4) job(10 * (i + 4) + a); }
        else if (i == 6) job(100 + a);
        else if (i == 7) job(113 + a);
        else if (i == 8) { if (a == 0) job(126); else bw[0][a - 1] = ldsc1(rs, boff + (a - 1) * 1024, s2 * 5120); }
        else if (a < 5) bw[2][a] = ldsc1(rs, boff + a * 1024, rotk<10>(2, s2) * 5120);
      };
      if (i == 9) {
        LAS f32x4* sp = (LAS f32x4*)(sm + ST2 + (32 * w + 8 * q) * 8);
        st0 = sp[0]; st1 = sp[1]; st2_ = sp[2]; st3 = sp[3];
#pragma unroll
        for (int t = 0; t < 5; ++t) Sc[t] = ((LAS float*)(sm + CSB))[16 * t + c];
        lr2 = f32x2{pm[c], pm[16 + c]}; li2 = f32x2{pm[40 + c], pm[56 + c]};
        lr4 = pm[32 + (c >> 1)]; ls4 = pm[72 + (c >> 1)];
        SCHED();
      }
#pragma unroll
      for (int m = 0; m < 14; ++m) { mmB(i & 1, m); if (i < 9) rdB((i + 1) & 1, i + 1, m); if (m & 1) jobA(m >> 1); SCHED(); }
      if (i < 9) {
        WAITL0();
        TS(1 + 3 * i);
        if (i < 6) waitv<10>();
        else if (i == 6) {
          asm volatile("s_waitcnt vmcnt(10)" : "+v"(efv[0]), "+v"(efv[1]) :: "memory");
          if (w == 1) {
            const unsigned tl = (unsigned)tag, th = (unsigned)(tag >> 32);
            const unsigned bad = (efv[0].x ^ tl) | (efv[0].y ^ th) | (efv[0].z ^ tl) | (efv[0].w ^ th) | (efv[1].x ^ tl) | (efv[1].y ^ th) | (efv[1].z ^ tl) | (efv[1].w ^ th);
            if (!__all(bad == 0u)) {
#pragma nounroll
              for (unsigned spins = 0; spins < SPIN_CAP; ++spins) {
                const v4u e0 = ldsc1(rs, FLAG_E_OFF + xcd * 2048 + lane * 32, 0), e1 = ldsc1(rs, FLAG_E_OFF + xcd * 2048 + lane * 32 + 16, 0);
                if (__all(((e0.x ^ tl) | (e0.y ^ th) | (e0.z ^ tl) | (e0.w ^ th) | (e1.x ^ tl) | (e1.y ^ th) | (e1.z ^ tl) | (e1.w ^ th)) == 0u)) break;
                asm volatile("s_sleep 2" ::: "memory");
              }
            }
            if (b == 0 && lane == 0) __hip_atomic_store((GAS u64*)(ws + EPOCH_OFF), tag, RLX_AGENT);
          }
        }
        else if (i == 7) waitv<13>();
        else waitv<24>();
        TS(2 + 3 * i);
        BAR();
        TS(3 + 3 * i);
      }
      if (i == 2 && tid < 8) __hip_atomic_store((GAS u64*)(ws + FLAG_E_OFF) + tid * 256 + b, tag, RLX_AGENT);
      if (i == 5) {
        asm volatile("s_cmp_eq_u32 %4, 1\n\ts_cselect_b64 exec, -1, 0\n\tglobal_load_dwordx4 %0, %2, %3 sc1\n\tglobal_load_dwordx4 %1, %2, %3 offset:16 sc1\n\ts_mov_b64 exec, -1"
                     : "=&v"(efv[0]), "=&v"(efv[1]) : "v"(l16 * 2), "s"(ws + FLAG_E_OFF + xcd * 2048), "s"(w) : "memory", "scc");
      }
      if (i == 8 && w == 3) {
        const int l48 = lane < 48 ? lane : 47;
        f32x2 a = *(LAS f32x2*)(sm + ESR + l48 * 8);
#pragma unroll
        for (int k = 1; k < 16; ++k) a += *(LAS f32x2*)(sm + ESR + k * 384 + l48 * 8);
        *(LAS f32x2*)(sm + CSF + l48 * 8) = a;
        lds_signal(cnt + 8, lane);
      }
      SCHED();
#pragma unroll
      for (int m = 14; m < 20; ++m) { mmB(i & 1, m); jobB(m - 14); SCHED(); }
    };
    stepB(IC(1)); stepB(IC(2)); stepB(IC(3)); stepB(IC(4)); stepB(IC(5)); stepB(IC(6)); stepB(IC(7)); stepB(IC(8)); stepB(IC(9));
    STAMP(1); TQ(0);
    __builtin_amdgcn_s_setprio(2);
    f32x2 ur[8], ui[8], er, ei;
    float u4[8], e4;
    ls4 = (c & 1) ? ls4 : -ls4;
    {
      const float rsa[4] = {st0[0], st0[2], st1[0], st1[2]}, rsb[4] = {st2_[0], st2_[2], st3[0], st3[2]};
      f32x2 mba[4] = {{st0[1], st0[1]}, {st0[3], st0[3]}, {st1[1], st1[1]}, {st1[3], st1[3]}}, mbb[4] = {{st2_[1], st2_[1]}, {st2_[3], st2_[3]}, {st3[1], st3[1]}, {st3[3], st3[3]}};
#pragma unroll
      for (int s = 0; s < 4; ++s) { asm volatile("" : "+v"(mba[s])); asm volatile("" : "+v"(mbb[s])); }
      const f32x2 Sr = {Sc[0], Sc[2]}, Si = {Sc[1], Sc[3]};
#pragma unroll
      for (int s = 0; s < 4; ++s) {
        ur[s] = (f32x2{acc[0][0][s], acc[0][2][s]} - mba[s] * Sr) * rsa[s]; ui[s] = (f32x2{acc[0][1][s], acc[0][3][s]} - mba[s] * Si) * rsa[s];
        ur[4 + s] = (f32x2{acc[1][0][s], acc[1][2][s]} - mbb[s] * Sr) * rsb[s]; ui[4 + s] = (f32x2{acc[1][1][s], acc[1][3][s]} - mbb[s] * Si) * rsb[s];
        u4[s] = (acc[0][4][s] - mba[s][0] * Sc[4]) * rsa[s]; u4[4 + s] = (acc[1][4][s] - mbb[s][0] * Sc[4]) * rsb[s];
      }
      er = ur[0]; ei = ui[0]; e4 = u4[0];
#pragma unroll
      for (int s = 1; s < 8; ++s) {
        const f32x2 nr = lr2 * er + (ur[s] - li2 * ei), ni = lr2 * ei + (li2 * er + ui[s]);
        er = nr; ei = ni;
        e4 = lr4 * e4 + (ls4 * swp1(e4) + u4[s]);
      }
    }
    lds_wait(cnt + 8, 1u);
    LAS float* es = (LAS float*)(sm + ESR);
    es[(4 * w + q) * 80 + 2 * c] = er[0]; es[(4 * w + q) * 80 + 2 * c + 1] = ei[0];
    es[(4 * w + q) * 80 + 32 + 2 * c] = er[1]; es[(4 * w + q) * 80 + 33 + 2 * c] = ei[1];
    es[(4 * w + q) * 80 + 64 + c] = e4;
    lds_signal(cnt + 0, lane);
    TQ(1);
    const int l40 = lane < 40 ? lane : 39;
    const unsigned tlo = (unsigned)tag >> 24 << 24, thi = (unsigned)(tag >> 32);
    if (w == 0) {
      const float l8r = pm[80 + l40], l8i = pm[120 + l40];
      lds_wait(cnt + 0, 4u);
      TQ(2);
      f32x2 ev[16];
#pragma unroll
      for (int s = 0; s < 16; ++s) ev[s] = *(LAS f32x2*)(es + s * 80 + 2 * l40);
      float Er = 0.f, Ei = 0.f;
#pragma unroll
      for (int s = 0; s < 16; ++s) {
        const float pr_ = Er, pi_ = Ei;
        Er = __builtin_fmaf(l8r, pr_, __builtin_fmaf(-l8i, pi_, ev[s][0])); Ei = __builtin_fmaf(l8r, pi_, __builtin_fmaf(l8i, pr_, ev[s][1]));
        asm volatile("" : "+v"(Er)); asm volatile("" : "+v"(Ei));
        ev[s] = f32x2{pr_, pi_};
      }
      if (r < 15 && lane < 40) {
        v4u o; o.x = tlo | ((__builtin_bit_cast(unsigned, Er) + 0x80u) >> 8); o.y = thi; o.z = tlo | ((__builtin_bit_cast(unsigned, Ei) + 0x80u) >> 8); o.w = thi;
        __builtin_amdgcn_raw_buffer_store_b128(o, rs, EG_OFF + ((j * 16 + r) * 40 + lane) * 16, 0, AUX_SC1);
      }
      STAMP(2); TQ(3);
#pragma unroll
      for (int s = 0; s < 16; ++s) *(LAS f32x2*)(es + s * 80 + 2 * l40) = ev[s];
    }
    waitv<15>();
    if (lane == 0) __hip_atomic_fetch_add(cnt + 4 + w, 1u, RLX_WG);
#pragma unroll
    for (int k = PBY; k < DBY; ++k)
#pragma unroll
      for (int n = 0; n < 5; ++n) bw[k][n] = ldsc1(rs, boff + n * 1024, rotk<10>(k, s2) * 5120);
    SCHED();
    f32x2 gr = {1.f, 1.f}, gi = {0.f, 0.f}, g4r = {1.f, 1.f}, g4i = {0.f, 0.f};
    { f32x2 pr = {pm[80 + c], pm[96 + c]}, pi = {pm[120 + c], pm[136 + c]};
      f32x2 qr = {pm[112 + (c >> 1)], 0.f}, qi = {pm[152 + (c >> 1)], 0.f};
      const int sg = 4 * w + q;
#pragma unroll
      for (int bt = 0; bt < 4; ++bt) {
        const f32x2 tr_ = gr * pr - gi * pi, ti_ = gr * pi + gi * pr, ur_ = g4r * qr - g4i * qi, ui_ = g4r * qi + g4i * qr;
        if ((sg >> bt) & 1) { gr = tr_; gi = ti_; g4r = ur_; g4i = ui_; }
        if (bt < 3) { const f32x2 n1 = pr * pr - pi * pi, n2 = qr * qr - qi * qi; pi = 2.f * pr * pi; pr = n1; qi = 2.f * qr * qi; qr = n2; }
      } }
    const float g4s = (c & 1) ? g4i[0] : -g4i[0];
    const f32x2 cr2 = f32x2{2.f * pm[240 + c], 2.f * pm[256 + c]}, ci2 = f32x2{2.f * pm[280 + c], 2.f * pm[296 + c]};
    const float cr4 = pm[272 + (c >> 1)], ca4 = (c & 1) ? -2.f * cr4 : 2.f * cr4, cb4 = -2.f * pm[312 + (c >> 1)];
    int hoffs[5];
#pragma unroll
    for (int m = 0; m < 5; ++m) {
      const int unit = 4 * m + g, kap = 10 * j + (unit >> 1);
      hoffs[m] = HT_OFF + (((2 * r + (w >> 1)) * 40 + (kap >> 2)) * 4 + 2 * (w & 1) + (unit & 1)) * 1024 + (kap & 3) * 256 + n16 * 16;
      asm volatile("" : "+v"(hoffs[m]));
    }
    TQ(4);
    if (w < 2) {
      float Hr_ = 0.f, Hi_ = 0.f;
      if (r > 8 * w) {
        const float lKr = pm[160 + l40], lKi = pm[200 + l40];
        float p4r, p4i, p8r, p8i;
        { const float a2r = lKr * lKr - lKi * lKi, a2i = 2.f * lKr * lKi; p4r = a2r * a2r - a2i * a2i; p4i = 2.f * a2r * a2i; p8r = p4r * p4r - p4i * p4i; p8i = 2.f * p4r * p4i; }
        const int eo = EG_OFF + (j * 16 * 40 + l40) * 16, top = r - 1 - 8 * w;
        v4u gv[8];
#pragma nounroll
        for (unsigned spins = 0; spins < SPIN_CAP; ++spins) {
#pragma unroll
          for (int m = 0; m < 8; ++m) gv[m] = ldsc1(rs, eo, (m <= top ? top - m : 0) * 640);
          unsigned bad = 0u, bad2 = 0u;
#pragma unroll
          for (int m = 0; m < 8; ++m) { bad |= (gv[m].x ^ tlo) | (gv[m].z ^ tlo); bad2 |= (gv[m].y ^ thi) | (gv[m].w ^ thi); }
          if (__all(((bad >> 24) | bad2) == 0u)) break;
          asm volatile("s_sleep 1" ::: "memory");
        }
        float hr_[2] = {0.f, 0.f}, hi_[2] = {0.f, 0.f};
#pragma unroll
        for (int m = 3; m >= 0; --m)
#pragma unroll
          for (int k = 0; k < 2; ++k) {
            const bool ok = 4 * k + m <= top;
            const float er_ = ok ? __builtin_bit_cast(float, gv[4 * k + m].x << 8) : 0.f, ei_ = ok ? __builtin_bit_cast(float, gv[4 * k + m].z << 8) : 0.f;
            const float nr_ = __builtin_fmaf(lKr, hr_[k], __builtin_fmaf(-lKi, hi_[k], er_)), ni_ = __builtin_fmaf(lKr, hi_[k], __builtin_fmaf(lKi, hr_[k], ei_));
            hr_[k] = nr_; hi_[k] = ni_;
            asm volatile("" : "+v"(hr_[k])); asm volatile("" : "+v"(hi_[k]));
          }
        Hr_ = p4r * hr_[1] - p4i * hi_[1] + hr_[0]; Hi_ = p4r * hi_[1] + p4i * hr_[1] + hi_[0];
        asm volatile("" : "+v"(Hr_)); asm volatile("" : "+v"(Hi_));
        if (w == 1) { const float t_ = p8r * Hr_ - p8i * Hi_; Hi_ = p8r * Hi_ + p8i * Hr_; Hr_ = t_; asm volatile("" : "+v"(Hr_)); asm volatile("" : "+v"(Hi_)); }
      }
      STAMP(3);
      ((LAS float*)(sm + HIN))[80 * w + 2 * l40] = Hr_; ((LAS float*)(sm + HIN))[80 * w + 2 * l40 + 1] = Hi_;
      lds_signal(cnt + 1, lane);
    }
    TQ(5);
    {
      lds_wait(cnt + 1, 2u);
      TQ(6);
      LAS unsigned char* scr = sm + w * 10240;
      LAS float* hin = (LAS float*)(sm + HIN);
      const f32x2 Hr = {hin[2 * c] + hin[80 + 2 * c], hin[32 + 2 * c] + hin[112 + 2 * c]}, Hi = {hin[2 * c + 1] + hin[81 + 2 * c], hin[33 + 2 * c] + hin[113 + 2 * c]};
      f32x2 hr = {es[(4 * w + q) * 80 + 2 * c], es[(4 * w + q) * 80 + 32 + 2 * c]}, hi = {es[(4 * w + q) * 80 + 2 * c + 1], es[(4 * w + q) * 80 + 33 + 2 * c]};
      hr += gr * Hr - gi * Hi; hi += gr * Hi + gi * Hr;
      float h4 = es[(4 * w + q) * 80 + 64 + c] + (g4r[0] * (hin[64 + c] + hin[144 + c]) + g4s * (hin[64 + (c ^ 1)] + hin[144 + (c ^ 1)])), hp4 = swp1(h4);
      f32x2 o2[2][8]; float o4[8];
#pragma unroll
      for (int s = 0; s < 8; ++s) {
        const f32x2 nr = lr2 * hr + (ur[s] - li2 * hi), ni = lr2 * hi + (li2 * hr + ui[s]);
        hr = nr; hi = ni;
        o2[0][s] = cr2 * hr - ci2 * hi;
        o2[1][s] = -(cr2 * hi + ci2 * hr);
        h4 = lr4 * h4 + (ls4 * hp4 + u4[s]); hp4 = swp1(h4);
        o4[s] = ca4 * h4 + cb4 * hp4;
      }
#pragma unroll
      for (int p = 0; p < 2; ++p)
#pragma unroll
        for (int pt = 0; pt < 2; ++pt) {
          v4u ov; ov.x = pk2(o2[pt][0][p], o2[pt][1][p]); ov.y = pk2(o2[pt][2][p], o2[pt][3][p]); ov.z = pk2(o2[pt][4][p], o2[pt][5][p]); ov.w = pk2(o2[pt][6][p], o2[pt][7][p]);
          *(LAS v4u*)(scr + (32 * p + 2 * c + pt) * 80 + q * 16) = ov;
        }
      { v4u ov; ov.x = pk2(o4[0], o4[1]); ov.y = pk2(o4[2], o4[3]); ov.z = pk2(o4[4], o4[5]); ov.w = pk2(o4[6], o4[7]);
        *(LAS v4u*)(scr + (64 + c) * 80 + q * 16) = ov; }
      WAITL0();
      TQ(7);
      LAS unsigned char* ra = scr + (8 * (g >> 1) + ((lane & 15) >> 2)) * 80 + (16 * (g & 1) + 4 * (lane & 3)) * 2;
      v2u lo[5], hi_[5];
#pragma unroll
      for (int m = 0; m < 5; ++m) { lo[m] = trread(ra + m * 1280); hi_[m] = trread(ra + m * 1280 + 320); }
#pragma unroll
      for (int m = 0; m < 5; ++m) {
        v4u ov; ov.x = lo[m].x; ov.y = lo[m].y; ov.z = hi_[m].x; ov.w = hi_[m].y;
        __builtin_amdgcn_raw_buffer_store_b128(ov, rs, hoffs[m], 0, AUX_SC1);
      }
    }
    STAMP(4); TQ(8);
    SCHED();
    waitv<0>();
    if (lane == 0) __hip_atomic_store((GAS u64*)(ws + FLAG_H_OFF) + ((r * 2 + (w >> 1)) * 16 + j) * 2 + (w & 1), tag, RLX_AGENT);
    STAMP(5); TQ(9);
    __builtin_amdgcn_s_setprio(0);
    TQ(10);
    poll_flags((const GAS u64*)(ws + FLAG_H_OFF) + ((r * 2 + half2) * 16 + 4 * w) * 2 + (lane & 7), tag);
    STAMP(6); TQ(11); TG(1);
    f32x4 ac2[5][4];
#pragma unroll
    for (int n = 0; n < 5; ++n)
#pragma unroll
      for (int i = 0; i < 4; ++i) ac2[n][i] = f32x4{0.f, 0.f, 0.f, 0.f};
    int aoff[4];
#pragma unroll
    for (int i = 0; i < 4; ++i) aoff[i] = HT_OFF + ((rt64 * 40 + 10 * w) * 4 + ((i + w) & 3)) * 1024 + lane * 16;
    const size_t orow = (size_t)(128 * r + 64 * half2 + 16 * w + n16) * D + 80 * ct + 4 * g;
    v2u xv[5];
    stream_gemm<10, DBY, DAY, NLY>(rs, boff, aoff, s2, bw, ac2,
        [&](int k) __attribute__((always_inline)) { return sm + yslot(w, k) + lane * 16; },
        [&]() __attribute__((always_inline)) {
#pragma unroll
          for (int n = 0; n < 5; ++n) xv[n] = *(const v2u*)(x + orow + 16 * n);
        }, [&](int k) __attribute__((always_inline)) { TG(2 + k); });
    STAMP(7); TQ(12);
    auto ypart = [&](int ww, int i) __attribute__((always_inline)) { return sm + (i < 2 ? ww * 10240 + i * 5120 : yslot(ww, 0)) + lane * 16; };
#pragma unroll
    for (int i = 1; i < 4; ++i)
#pragma unroll
      for (int n = 0; n < 5; ++n) *(LAS f32x4*)(ypart(w, i - 1) + n * 1024) = ac2[n][i];
    lds_signal(cnt + 3, lane);
    TQ(13);
    lds_wait(cnt + 10 + w, 1u);
    v4u gs[5];
#pragma unroll
    for (int n = 0; n < 5; ++n) {
      const v2u gq = *(LAS v2u*)(sm + fslot(w, 0) + n * 512 + lane * 8), sq = *(LAS v2u*)(sm + SKR + w * 2560 + n * 512 + lane * 8);
      gs[n] = v4u{gq.x, gq.y, sq.x, sq.y};
    }
    TQ(14);
    lds_wait(cnt + 3, 4u);
    TQ(15);
#pragma unroll
    for (int n = 0; n < 5; ++n) {
      f32x4 y = ac2[n][0] + f32x4{bf2f(gs[n].z & 0xffffu), bfhi(gs[n].z), bf2f(gs[n].w & 0xffffu), bfhi(gs[n].w)};
#pragma unroll
      for (int o = 1; o < 4; ++o) y += *(LAS f32x4*)(ypart((w + o) & 3, 3 - o) + n * 1024);
      const f32x4 gg = {bf2f(gs[n].x & 0xffffu), bfhi(gs[n].x), bf2f(gs[n].y & 0xffffu), bfhi(gs[n].y)};
      const f32x4 xx = {bf2f(xv[n].x & 0xffffu), bfhi(xv[n].x), bf2f(xv[n].y & 0xffffu), bfhi(xv[n].y)};
      *(f32x4*)(out + orow + 16 * n) = xx + y * gg;
    }
    STAMP(8); TQ(16);
    TS_OUT();
  } else {
    const int t32 = lane & 31, hh = lane >> 5;
    const int fx = (((t32 >> 3) & 3) << 1) | ((t32 >> 1) & 1);
    const int xa0 = XS + (32 * w + t32) * 128 + ((hh ^ fx) << 4);
    const int wa0 = W1S + (8 * hh + ((lane & 15) >> 2)) * 192 + (16 * ((lane >> 4) & 1) + 4 * (lane & 3)) * 2;
    f32x16 acc[3];
#pragma unroll
    for (int n = 0; n < 3; ++n)
#pragma unroll
      for (int e = 0; e < 16; ++e) acc[n][e] = 0.f;
    v4u xf[2][4]; v2u wf[2][4][3][2];
    auto rdF = [&](int set, int st, int q) __attribute__((always_inline)) {
      LAS unsigned char* sb = sm + (st % 3) * STAGE;
      const int s = q / 7, e = q % 7;
      if (e == 0) xf[set][s] = *(LAS v4u*)(sb + (xa0 ^ (s * 32)));
      else wf[set][s][(e - 1) >> 1][(e - 1) & 1] = trread(sb + wa0 + s * 3072 + ((e - 1) & 1) * 768 + ((e - 1) >> 1) * 64);
    };
    auto wfrag = [&](int set, int s, int n) __attribute__((always_inline)) { v4u v; v.x = wf[set][s][n][0].x; v.y = wf[set][s][n][0].y; v.z = wf[set][s][n][1].x; v.w = wf[set][s][n][1].y; return v; };
    auto mmF = [&](int set, int m) __attribute__((always_inline)) { const int s = m / 3, n = m % 3; acc[n] = mfma32(wfrag(set, s, n), xf[set][s], acc[n]); };
    BAR();
    STAMP(9);
#pragma unroll
    for (int q = 0; q < 28; ++q) rdF(0, 0, q);
    SCHED();
#pragma unroll
    for (int m = 0; m < 12; ++m) { mmF(0, m); SCHED(); }
    BAR();
    const unsigned ones = 0x3f803f80u;
    {
      {
        LAS unsigned char* xp = sm + XROW + (2 * w + hh) * 1280;
        const v4u xa = *(LAS v4u*)(xp + t32 * 16), xb = *(LAS v4u*)(xp + 512 + t32 * 16); const v2u xc = *(LAS v2u*)(xp + 1024 + t32 * 8);
        const unsigned xw[10] = {xa.x, xa.y, xa.z, xa.w, xb.x, xb.y, xb.z, xb.w, xc.x, xc.y};
        f32x2 xv2[10], a2 = {0.f, 0.f};
#pragma unroll
        for (int k = 0; k < 10; ++k) { xv2[k] = f32x2{bf2f(xw[k] & 0xffffu), bfhi(xw[k])}; a2 += xv2[k]; }
        const float mean = sum32(a2[0] + a2[1]) * (1.f / D);
        f32x2 q2 = {0.f, 0.f};
#pragma unroll
        for (int k = 0; k < 10; ++k) { const f32x2 d0 = xv2[k] - mean; q2 += d0 * d0; }
        const float var = sum32(q2[0] + q2[1]) * (1.f / D);
        const f32x2 st = {__builtin_amdgcn_rcpf(rbf(__builtin_amdgcn_sqrtf(rbf(rbf(var) + 1e-5f)))), rbf(mean)};
        if (t32 == 0) __builtin_amdgcn_raw_buffer_store_b64(__builtin_bit_cast(v2u, st), rs, PRS_OFF + (r * 128 + 8 * j + 2 * w + hh) * 8, 0, AUX_SC1);
        SCHED();
      }
      {
        if (w < 3) {
          const int br = 2 * w + hh > 4 ? 4 : 2 * w + hh;
          LAS unsigned char* xp = sm + BROW + br * 1280;
          const v4u xa = *(LAS v4u*)(xp + t32 * 16), xb = *(LAS v4u*)(xp + 512 + t32 * 16); const v2u xc = *(LAS v2u*)(xp + 1024 + t32 * 8);
          const int gl = (lane >> 4) & 1, f16 = 3 * fu + w;
          v2u lo[3], hi[3];
#pragma unroll
          for (int q3 = 0; q3 < 3; ++q3) {
            const int nt = q3 < 2 ? 2 * q3 + hh : 4;
            LAS unsigned char* ra = sm + W2SCR + (16 * w + 4 * gl + ((lane & 15) >> 2)) * 160 + (16 * nt + 4 * (lane & 3)) * 2;
            lo[q3] = trread(ra); hi[q3] = trread(ra + 8 * 160);
          }
          const float c1 = sum32(dot2(xc.y, ones, dot2(xc.x, ones, sum8(xb, sum8(xa, 0.f)))));
          const int chn = 5 * (r & 7) + br, part = r >> 3;
          const int col = chn < 32 ? 32 * (chn >> 4) + 16 * part + (chn & 15) : 2 * chn + part;
          if (t32 == 0) __builtin_amdgcn_raw_buffer_store_b32(__builtin_bit_cast(unsigned, c1), rs, PCB_OFF + (j * 256 + col) * 4, 0, AUX_SC1);
#pragma unroll
          for (int q3 = 0; q3 < 3; ++q3) {
            const int nt = q3 < 2 ? 2 * q3 + hh : 4;
            v4u ov; ov.x = lo[q3].x; ov.y = lo[q3].y; ov.z = hi[q3].x; ov.w = hi[q3].y;
            __builtin_amdgcn_raw_buffer_store_b128(ov, rs, WT_OFF + ((ctu * 88 + 40 + (f16 >> 1)) * 5 + nt) * 1024 + (2 * (f16 & 1) + gl) * 256 + n16 * 16, 0, AUX_SC1);
          }
        } else {
          float cs6[6] = {0.f, 0.f, 0.f, 0.f, 0.f, 0.f};
#pragma unroll
          for (int m = 0; m < 3; ++m) {
            const int kq = m < 2 ? g + 4 * m : 8 + (g & 1);
            const unsigned wgt = (m < 2 || g < 2) ? ones : 0u;
            LAS unsigned char* ra = sm + W1SCR + (4 * kq + ((lane & 15) >> 2)) * 192 + 8 * (lane & 3);
#pragma unroll
            for (int c6 = 0; c6 < 6; ++c6) { const v2u v = trread(ra + c6 * 32); cs6[c6] = dot2(v.y, wgt, dot2(v.x, wgt, cs6[c6])); }
          }
#pragma unroll
          for (int c6 = 0; c6 < 6; ++c6) cs6[c6] = add32(add16(cs6[c6]));
          const float v0 = g == 0 ? cs6[0] : g == 1 ? cs6[1] : g == 2 ? cs6[2] : cs6[3], v1 = (g & 1) ? cs6[5] : cs6[4];
          const int po = PCF_OFF + (j * 16 + r) * 384;
          __builtin_amdgcn_raw_buffer_store_b32(__builtin_bit_cast(unsigned, v0), rs, po + lane * 4, 0, AUX_SC1);
          __builtin_amdgcn_raw_buffer_store_b32(__builtin_bit_cast(unsigned, v1), rs, po + 256 + t32 * 4, 0, AUX_SC1);
        }
        SCHED();
      }
      {
        LAS u16* raw = (LAS u16*)(sm + RAW);
        if (w == 0) {
          const int l40 = lane < 40 ? lane : 39;
          const float dt = rbf(__expf(bf2f(raw[80 + l40])));
          const float lr = bf2f(raw[l40]), li = bf2f(raw[40 + l40]);
          const float mag = __expf(dt * lr);
          float rev = dt * li * 0.15915494309189535f; rev -= floorf(rev);
          const float ar = mag * __builtin_amdgcn_cosf(rev), ai = mag * __builtin_amdgcn_sinf(rev);
          const float nr = ar - 1.f, den = __builtin_amdgcn_rcpf(lr * lr + li * li);
          const float cr = (nr * lr + ai * li) * den, ci = (ai * lr - nr * li) * den;
          float pr = ar, pi = ai;
#pragma unroll
          for (int s = 0; s < 3; ++s) { const float t = pr * pr - pi * pi; pi = 2.f * pr * pi; pr = t; }
          const float p8r = pr, p8i = pi;
#pragma unroll
          for (int s = 0; s < 4; ++s) { const float t = pr * pr - pi * pi; pi = 2.f * pr * pi; pr = t; }
          LAS float* pm = (LAS float*)(sm + PRM) + l40;
          pm[0] = ar; pm[40] = ai; pm[80] = p8r; pm[120] = p8i; pm[160] = pr; pm[200] = pi; pm[240] = cr; pm[280] = ci;
        } else if (w == 1) {
          const int l48 = lane < 48 ? lane : 47;
          const unsigned v = *(LAS unsigned*)(sm + RAW + 240 + l48 * 4);
          *(LAS f32x2*)(sm + B1F + l48 * 8) = f32x2{bf2f(v & 0xffffu), bfhi(v)};
        } else {
          const int l40 = lane < 40 ? lane : 39;
          const unsigned v = *(LAS unsigned*)(sm + RAW + 432 + (w - 2) * 160 + l40 * 4);
          *(LAS f32x2*)(sm + DB2 + (w - 2) * 320 + l40 * 8) = f32x2{bf2f(v & 0xffffu), bfhi(v)};
        }
        SCHED();
      }
    }
    TS(1); TS(2); BAR(); TS(3);
#pragma unroll
    for (int q = 0; q < 28; ++q) rdF(1, 1, q);
    WAITL0(); BAR();
    const u64 tag = read_tag();
    v4u bw[DBF][5];
    const int boff = WT_OFF + (ct * 88 + 40 + 12 * w) * 5120 + lane * 16;
    const int s2 = s2f;
    auto stepF = [&](auto I_) __attribute__((always_inline)) {
      constexpr int i = decltype(I_)::value;
#pragma unroll
      for (int m = 0; m < 8; ++m) {
        mmF(i & 1, m);
        if (i < 9) {
#pragma unroll
          for (int q = (m < 4 ? 4 * m : 3 * m + 4); q < (m < 4 ? 4 * m + 4 : 3 * m + 7); ++q) rdF((i + 1) & 1, i + 1, q);
        }
        SCHED();
      }
      if (i < 9) {
        WAITL0();
        TS(1 + 3 * i);
        if (i == 2) waitv<0>();
        TS(2 + 3 * i);
        BAR();
        TS(3 + 3 * i);
      }
      SCHED();
#pragma unroll
      for (int m = 8; m < 12; ++m) {
        mmF(i & 1, m);
        if constexpr (i >= 8) {
          bw[i - 8][m - 8] = ldsc1(rs, boff + (m - 8) * 1024, rotk<12>(i - 8, s2) * 5120);
          if (m == 11) bw[i - 8][4] = ldsc1(rs, boff + 4096, rotk<12>(i - 8, s2) * 5120);
        }
        SCHED();
      }
    };
    stepF(IC(1)); stepF(IC(2)); stepF(IC(3)); stepF(IC(4)); stepF(IC(5)); stepF(IC(6)); stepF(IC(7)); stepF(IC(8)); stepF(IC(9));
    STAMP(1); TQ(0);
    {
      lds_wait(cnt + 8, 1u);
      const f32x2 st2 = *(LAS f32x2*)(sm + ST2 + (32 * w + t32) * 8);
      f32x4 S4[12], b4[12];
#pragma unroll
      for (int k = 0; k < 12; ++k) { S4[k] = *(LAS f32x4*)(sm + CSF + (8 * k + 4 * hh) * 4); b4[k] = *(LAS f32x4*)(sm + B1F + (8 * k + 4 * hh) * 4); }
      const float rsd = st2[0], nm = -rsd * st2[1];
      const int hoff = H1T_OFF + (((2 * r + (w >> 1)) * 48 + 3 * j) * 4 + 2 * (w & 1) + (t32 >> 4)) * 1024 + hh * 256 + (t32 & 15) * 16;
#pragma unroll
      for (int n = 0; n < 3; ++n) {
        unsigned ou[8];
#pragma unroll
        for (int e = 0; e < 8; ++e) {
          const f32x2 a = {acc[n][2 * e], acc[n][2 * e + 1]};
          const f32x4 S = S4[4 * n + (e >> 1)], bb = b4[4 * n + (e >> 1)];
          const f32x2 S2 = {S[2 * (e & 1)], S[2 * (e & 1) + 1]}, b2_ = {bb[2 * (e & 1)], bb[2 * (e & 1) + 1]};
          const f32x2 v = rsd * a + (nm * S2 + b2_);
          const f32x2 z = v * (-2.302208198f - 0.1029432397f * (v * v));
          f32x2 d = {__builtin_amdgcn_exp2f(z[0]), __builtin_amdgcn_exp2f(z[1])};
          d += 1.f;
          const f32x2 rc = {__builtin_amdgcn_rcpf(d[0]), __builtin_amdgcn_rcpf(d[1])};
          const f32x2 gl = v * rc;
          ou[e] = pk2(gl[0], gl[1]);
        }
        __builtin_amdgcn_raw_buffer_store_b128(v4u{ou[0], ou[1], ou[2], ou[3]}, rs, hoff + n * 4096, 0, AUX_SC1);
        __builtin_amdgcn_raw_buffer_store_b128(v4u{ou[4], ou[5], ou[6], ou[7]}, rs, hoff + n * 4096 + 512, 0, AUX_SC1);
        SCHED();
        TQ(1 + n);
      }
    }
    STAMP(4);
    v2u xv[5];
    { const u16* xp = x + (size_t)(128 * r + 64 * half2 + 16 * w + n16) * D + 80 * ct + 4 * g;
#pragma unroll
      for (int n = 0; n < 5; ++n) xv[n] = *(const v2u*)(xp + 16 * n); }
    SCHED();
    waitv<5>();
    if (lane == 0) __hip_atomic_store((GAS u64*)(ws + FLAG_F_OFF) + ((r * 2 + (w >> 1)) * 16 + j) * 2 + (w & 1), tag, RLX_AGENT);
    STAMP(5); TQ(4);
#pragma unroll
    for (int k = PBF; k < DBF; ++k)
#pragma unroll
      for (int n = 0; n < 5; ++n) bw[k][n] = ldsc1(rs, boff + n * 1024, rotk<12>(k, s2) * 5120);
    const int trow = 64 * half2 + 16 * w + n16;
    {
      float est[2] = {((LAS float*)(sm + ST2))[2 * trow], ((LAS float*)(sm + ST2))[2 * trow + 1]};
      asm volatile("" : "+v"(est[0])); asm volatile("" : "+v"(est[1]));
#pragma unroll
      for (int n = 0; n < 5; ++n) {
        const f32x4 ds = *(LAS f32x4*)(sm + DB2 + (16 * n + 4 * g) * 4);
        const unsigned d0 = pk2(bf2f(xv[n].x & 0xffffu) - est[1], bfhi(xv[n].x) - est[1]), d1 = pk2(bf2f(xv[n].y & 0xffffu) - est[1], bfhi(xv[n].y) - est[1]);
        const unsigned n0 = pk2(bf2f(d0 & 0xffffu) * est[0], bfhi(d0) * est[0]), n1 = pk2(bf2f(d1 & 0xffffu) * est[0], bfhi(d1) * est[0]);
        *(LAS v2u*)(sm + SKR + w * 2560 + n * 512 + lane * 8) = v2u{pk2(bf2f(n0 & 0xffffu) * ds[0], bfhi(n0) * ds[1]), pk2(bf2f(n1 & 0xffffu) * ds[2], bfhi(n1) * ds[3])};
      }
    }
    TQ(5);
    poll_flags((const GAS u64*)(ws + FLAG_F_OFF) + ((r * 2 + half2) * 16 + 4 * w) * 2 + (lane & 7), tag);
    STAMP(6); TQ(6); TG(1);
    f32x4 ac2[5][4];
#pragma unroll
    for (int n = 0; n < 5; ++n)
#pragma unroll
      for (int i = 0; i < 4; ++i) ac2[n][i] = f32x4{0.f, 0.f, 0.f, 0.f};
    int aoff[4];
#pragma unroll
    for (int i = 0; i < 4; ++i) aoff[i] = H1T_OFF + ((rt64 * 48 + 12 * w) * 4 + ((i + w) & 3)) * 1024 + lane * 16;
    __builtin_amdgcn_s_setprio(1);
    stream_gemm<12, DBF, DAF, NLF>(rs, boff, aoff, s2, bw, ac2,
        [&](int k) __attribute__((always_inline)) { if (k == 0) lds_wait(cnt + 4 + w, 1u); return sm + fslot(w, k) + lane * 16; },
        [&]() __attribute__((always_inline)) {}, [&](int k) __attribute__((always_inline)) { TG(2 + k); });
    STAMP(7); TQ(7);
#pragma unroll
    for (int i = 1; i < 4; ++i)
#pragma unroll
      for (int n = 0; n < 5; ++n) *(LAS f32x4*)(sm + fslot(w, i - 1) + n * 1024 + lane * 16) = ac2[n][i];
    lds_signal(cnt + 2, lane);
    v2u gs[5];
    TQ(8);
    lds_wait(cnt + 2, 4u);
    TQ(9);
    f32x4 h2[5];
#pragma unroll
    for (int n = 0; n < 5; ++n) {
      h2[n] = ac2[n][0];
#pragma unroll
      for (int o = 1; o < 4; ++o) h2[n] += *(LAS f32x4*)(sm + fslot((w + o) & 3, 3 - o) + n * 1024 + lane * 16);
    }
    lds_signal(cnt + 9, lane);
#pragma unroll
    for (int n = 0; n < 5; ++n) {
      const f32x4 bs = *(LAS f32x4*)(sm + DB2 + 320 + (16 * n + 4 * g) * 4);
      const unsigned r0 = pk2(h2[n][0], h2[n][1]), r1 = pk2(h2[n][2], h2[n][3]);
      const unsigned a0 = pk2(bf2f(r0 & 0xffffu) + bs[0], bfhi(r0) + bs[1]), a1 = pk2(bf2f(r1 & 0xffffu) + bs[2], bfhi(r1) + bs[3]);
      const float a[4] = {bf2f(a0 & 0xffffu), bfhi(a0), bf2f(a1 & 0xffffu), bfhi(a1)};
      float sg[4];
#pragma unroll
      for (int e = 0; e < 4; ++e) sg[e] = a[e] * __builtin_amdgcn_rcpf(1.f + __builtin_amdgcn_exp2f(-1.4426950408889634f * a[e]));
      gs[n].x = pk2(sg[0], sg[1]); gs[n].y = pk2(sg[2], sg[3]);
      asm volatile("" : "+v"(gs[n]));
    }
    STAMP(2); TQ(10);
    lds_wait(cnt + 9, 4u);
#pragma unroll
    for (int n = 0; n < 5; ++n) *(LAS v2u*)(sm + fslot(w, 0) + n * 512 + lane * 8) = gs[n];
    lds_signal(cnt + 10 + w, lane);
    STAMP(8); TQ(11);
    TS_OUT();
  }
}
}

extern "C" void kernel_launch(void* const* d_in, const int* in_sizes, int n_in,
                              void* d_out, int out_size, void* d_ws, size_t ws_size,
                              hipStream_t stream) {
  using s5k::u16;
  (void)hipFuncSetAttribute((const void*)s5k::s5_fused, hipFuncAttributeMaxDynamicSharedMemorySize, s5k::LDS_BYTES);
  s5k::s5_fused<<<s5k::NWG, s5k::NTHR, s5k::LDS_BYTES, stream>>>(
      (const u16*)d_in[0], (const u16*)d_in[1], (const u16*)d_in[2], (const u16*)d_in[3], (const u16*)d_in[4], (const u16*)d_in[5],
      (const u16*)d_in[6], (const u16*)d_in[7], (const u16*)d_in[8], (const u16*)d_in[10], (const u16*)d_in[11], (const u16*)d_in[12],
      (const u16*)d_in[13], (float*)d_out, (unsigned char*)d_ws);
}
```

```cpp
#include <hip/hip_runtime.h>
#include <hip/hip_bf16.h>
#include <stdint.h>
#include <type_traits>

namespace s5k {
typedef unsigned short u16;
typedef unsigned long long u64;
typedef unsigned v4u __attribute__((ext_vector_type(4)));
typedef unsigned v2u __attribute__((ext_vector_type(2)));
typedef float f32x4 __attribute__((ext_vector_type(4)));
typedef float f32x2 __attribute__((ext_vector_type(2)));
typedef float f32x16 __attribute__((ext_vector_type(16)));
typedef __bf16 bf16x8 __attribute__((ext_vector_type(8)));
typedef __bf16 bf16x2 __attribute__((ext_vector_type(2)));
typedef short s16x4 __attribute__((ext_vector_type(4)));
#define WAVE_ALL(c) (__builtin_amdgcn_ballot_w64(!(c)) == 0ull)
#define GAS __attribute__((address_space(1)))
#define LAS __attribute__((address_space(3)))
#define RLX_AGENT __ATOMIC_RELAXED, __HIP_MEMORY_SCOPE_AGENT
#define RLX_WG __ATOMIC_RELAXED, __HIP_MEMORY_SCOPE_WORKGROUP

constexpr int L = 2048, D = 640, P = 640, F = 1536;
constexpr int NWG = 256, NTHR = 512;
constexpr int EPOCH_OFF = 0;
constexpr int FLAG_E_OFF = 4096;
constexpr int FLAG_F_OFF = 65536;
constexpr int FLAG_H_OFF = 65536 + 8192;
constexpr int EG_OFF = 131072;
constexpr int PRS_OFF = 512 << 10;
constexpr int PCB_OFF = 576 << 10;
constexpr int PCF_OFF = 640 << 10;
constexpr int WT_OFF = 1 << 20;
constexpr int H1T_OFF = 8 << 20;
constexpr int HT_OFF = 16 << 20;
constexpr int WS_BYTES = 24 << 20;
constexpr int XS = 0, BWS = 16384, W1S = 26624, STAGE = 38912;
constexpr int XR = 3 * STAGE;
constexpr int XROW = XR, BROW = XR + 10240, W1SCR = BROW + 6400, W2SCR = W1SCR + 7680, CSCR = W2SCR + 7680, RAW = CSCR + 8192;
constexpr int MISC = 158976;
static_assert(RAW + 1024 <= MISC, "");
constexpr int ST2 = MISC;
constexpr int CSB = MISC + 1024;
constexpr int CSF = MISC + 2048;
constexpr int B1F = MISC + 2432;
constexpr int PRM = MISC + 2816;
constexpr int CNT = MISC + 4096;
constexpr int DB2 = MISC + 4160;
constexpr int HIN = CSB + 384;
constexpr int LDS_BYTES = MISC + 4864;
static_assert(HIN + 640 <= LDS_BYTES, "");
constexpr int SLOT0 = 40960;
constexpr int ESR = SLOT0 + 20 * 5120;
__device__ __forceinline__ int yslot(int w, int k) { return SLOT0 + (k ? 11 + w : (w == 3 ? 19 : 4 + w)) * 5120; }
__device__ __forceinline__ int fslot(int w, int k) { return SLOT0 + (k == 0 ? 15 + w : k == 1 ? w : 7 + w) * 5120; }
constexpr int SKR = ESR + 5120;
constexpr int JUNK = SKR + 10240;
static_assert(JUNK + 256 <= MISC, "");
static_assert(LDS_BYTES <= 163840, "");
constexpr unsigned SPIN_CAP = 1u << 16;
constexpr int AUX_SC1 = 16;

__device__ __forceinline__ float bf2f(unsigned b) { return __builtin_bit_cast(float, b << 16); }
__device__ __forceinline__ float bfhi(unsigned b) { return __builtin_bit_cast(float, b & 0xffff0000u); }
__device__ __forceinline__ unsigned pk2(float a, float b) { f32x2 f = {a, b}; return __builtin_bit_cast(unsigned, __builtin_convertvector(f, bf16x2)); }
__device__ __forceinline__ float rbf(float a) { return bf2f(pk2(a, a) & 0xffffu); }
__device__ __forceinline__ float dot2(unsigned a, unsigned b, float c) {
  return __builtin_amdgcn_fdot2_f32_bf16(__builtin_bit_cast(bf16x2, a), __builtin_bit_cast(bf16x2, b), c, false);
}
__device__ __forceinline__ float sum8(v4u a, float c) {
  const unsigned ones = 0x3f803f80u;
  c = dot2(a.x, ones, c); c = dot2(a.y, ones, c); c = dot2(a.z, ones, c); c = dot2(a.w, ones, c); return c;
}
__device__ __forceinline__ float sq8(v4u a, float c) {
  c = dot2(a.x, a.x, c); c = dot2(a.y, a.y, c); c = dot2(a.z, a.z, c); c = dot2(a.w, a.w, c); return c;
}
__device__ __forceinline__ float swp1(float v) {
  return __builtin_bit_cast(float, __builtin_amdgcn_update_dpp(0, __builtin_bit_cast(int, v), 0xB1, 0xF, 0xF, true));
}
template <int CTRL> __device__ __forceinline__ float dppf(float v) {
  return __builtin_bit_cast(float, __builtin_amdgcn_update_dpp(0, __builtin_bit_cast(int, v), CTRL, 0xF, 0xF, true));
}
__device__ __forceinline__ float add16(float v) {
  float c_ = v;
  asm("s_nop 1\n\tv_permlane16_swap_b32 %0, %1" : "+v"(v), "+v"(c_));
  return v + c_;
}
__device__ __forceinline__ float add32(float v) {
  float c_ = v;
  asm("s_nop 1\n\tv_permlane32_swap_b32 %0, %1" : "+v"(v), "+v"(c_));
  return v + c_;
}
__device__ __forceinline__ float sum32(float v) {
  v += dppf<0xB1>(v); v += dppf<0x4E>(v); v += dppf<0x141>(v); v += dppf<0x140>(v); return add16(v);
}
__device__ __forceinline__ f32x2 cmad(f32x2 lrr, f32x2 lsi, f32x2 h, f32x2 u) {
  return lrr * h + (lsi * __builtin_shufflevector(h, h, 1, 0) + u);
}
__device__ __forceinline__ f32x4 mfma16(v4u a, v4u b, f32x4 c) {
  return __builtin_amdgcn_mfma_f32_16x16x32_bf16(__builtin_bit_cast(bf16x8, a), __builtin_bit_cast(bf16x8, b), c, 0, 0, 0);
}
__device__ __forceinline__ f32x16 mfma32(v4u a, v4u b, f32x16 c) {
  return __builtin_amdgcn_mfma_f32_32x32x16_bf16(__builtin_bit_cast(bf16x8, a), __builtin_bit_cast(bf16x8, b), c, 0, 0, 0);
}
__device__ __forceinline__ v2u trread(LAS unsigned char* p) {
  return __builtin_bit_cast(v2u, __builtin_amdgcn_ds_read_tr16_b64_v4i16((LAS s16x4*)p));
}
__device__ __forceinline__ void dma1(const void* gb, unsigned vo, unsigned ld) {
  asm volatile("s_mov_b32 m0, %2\n\ts_nop 0\n\tglobal_load_lds_dwordx4 %1, %0" :: "s"(gb), "v"(vo), "s"(ld) : "memory");
}
__device__ __forceinline__ void dma1h(const void* gb, unsigned vo, unsigned ld) {
  asm volatile("s_mov_b32 m0, %2\n\ts_mov_b32 exec_hi, 0\n\tglobal_load_lds_dwordx4 %1, %0\n\ts_mov_b32 exec_hi, -1" :: "s"(gb), "v"(vo), "s"(ld) : "memory");
}
__device__ __forceinline__ void dma1s(const void* gb, unsigned vo, unsigned ld) {
  asm volatile("s_mov_b32 m0, %2\n\ts_nop 0\n\tglobal_load_lds_dwordx4 %1, %0 sc1" :: "s"(gb), "v"(vo), "s"(ld) : "memory");
}
template <int N> __device__ __forceinline__ void waitv() { asm volatile("s_waitcnt vmcnt(%0)" :: "i"(N) : "memory"); }
#define SCHED() __builtin_amdgcn_sched_barrier(0)
#define ABL_ST(x) x
#define WAITL0() asm volatile("s_waitcnt lgkmcnt(0)" ::: "memory")
#define BAR() __builtin_amdgcn_s_barrier()
#define LDU16(dst, ptr) asm volatile("global_load_ushort %0, %1, off" : "=v"(dst) : "v"(ptr) : "memory")
#define LDU32(dst, ptr) asm volatile("global_load_dword %0, %1, off" : "=v"(dst) : "v"(ptr) : "memory")
#define LDU64(dst, ptr) asm volatile("global_load_dwordx2 %0, %1, off" : "=v"(dst) : "v"(ptr) : "memory")
#define LDU128(dst, ptr) asm volatile("global_load_dwordx4 %0, %1, off" : "=v"(dst) : "v"(ptr) : "memory")

#define STAMP(k) do {} while (0)
#define TS(k) do {} while (0)
#define TQ(k) do {} while (0)
#define TG(k) do {} while (0)
#define TS_OUT() do {} while (0)

__device__ __forceinline__ u64 mix64(u64 z) {
  z += 0x9E3779B97F4A7C15ull; z = (z ^ (z >> 30)) * 0xBF58476D1CE4E5B9ull; z = (z ^ (z >> 27)) * 0x94D049BB133111EBull; return z ^ (z >> 31);
}
__device__ __forceinline__ void poll_flags(const GAS u64* f, u64 tag) {
#pragma nounroll
  for (unsigned spins = 0; spins < SPIN_CAP; ++spins) {
    const u64 v = __hip_atomic_load(f, RLX_AGENT);
    if (WAVE_ALL(v == tag)) break;
    __builtin_amdgcn_s_sleep(1);
  }
  __builtin_amdgcn_fence(__ATOMIC_ACQUIRE, "wavefront");
}
__device__ __forceinline__ void lds_signal(LAS unsigned* c, int lane) {
  WAITL0();
  if (lane == 0) __hip_atomic_fetch_add(c, 1u, RLX_WG);
}
__device__ __forceinline__ void lds_wait(LAS unsigned* c, unsigned want) {
#pragma nounroll
  for (unsigned spins = 0; spins < SPIN_CAP * 16; ++spins) {
    if (__hip_atomic_load(c, RLX_WG) >= want) break;
    __builtin_amdgcn_s_sleep(1);
  }
  asm volatile("" ::: "memory");
}
template <int NK> __device__ __forceinline__ int rotk(int k, int s) { const int a = k + s; return a >= NK ? a - NK : a; }
__device__ __forceinline__ v4u ldsc1(__amdgpu_buffer_rsrc_t rs, int voff, int soff) { return __builtin_amdgcn_raw_buffer_load_b128(rs, voff, soff, AUX_SC1); }

template <int NK, int DB, int DA, int NL, class LW, class HK, class HK2>
__device__ __forceinline__ void stream_gemm(__amdgpu_buffer_rsrc_t rs, int boff, const int (&aoff)[4], int s2, v4u (&bw)[DB][5], f32x4 (&acc)[5][4], LW lw, HK hook, HK2 hk2) {
  v4u aw[DA][4];
#pragma unroll
  for (int k = 0; k < DA; ++k) {
#pragma unroll
    for (int i = 0; i < 4; ++i) aw[k][i] = ldsc1(rs, aoff[i], rotk<NK>(k, s2) * 4096);
    SCHED();
  }
#pragma unroll
  for (int k = 0; k < NK; ++k) {
#pragma unroll
    for (int i = 0; i < 4; ++i) {
#pragma unroll
      for (int n = 0; n < 5; ++n) acc[n][i] = mfma16(bw[k % DB][n], aw[k % DA][i], acc[n][i]);
      SCHED();
      if (k + DA < NK) aw[k % DA][i] = ldsc1(rs, aoff[i], rotk<NK>(k + DA, s2) * 4096);
      SCHED();
    }
    if (k == NK - DA) hook();
    hk2(k);
    if (k + DB < NK) {
      if (k < NL) {
        LAS unsigned char* lp = lw(k);
#pragma unroll
        for (int n = 0; n < 5; ++n) bw[k % DB][n] = *(LAS v4u*)(lp + n * 1024);
      } else {
#pragma unroll
        for (int n = 0; n < 5; ++n) bw[k % DB][n] = ldsc1(rs, boff + n * 1024, rotk<NK>(k + DB, s2) * 5120);
      }
    }
    SCHED();
  }
}

#define ROT1 2
#define ROT2 0
#define DAF_ 3
#define DAY_ 3
constexpr int DBF = 5, DAF = DAF_, DBY = 5, DAY = DAY_, NLF = 3, NLY = 2;
constexpr int PBF = 2, PBY = 3;
__device__ __forceinline__ void dma1m(const void* gb, unsigned vo, unsigned ld, int sh) {
  asm volatile("s_mov_b32 m0, %2\n\ts_lshr_b64 exec, exec, %3\n\tglobal_load_lds_dwordx4 %1, %0\n\ts_mov_b64 exec, -1" :: "s"(gb), "v"(vo), "s"(ld), "s"(sh) : "memory", "scc");
}
constexpr int CODE_LINES = 344;
#define IC(n) std::integral_constant<int, n>{}

#if defined(__HIP_DEVICE_COMPILE__) && !defined(USE_PK)
#define NOPK_ATTR __attribute__((target("no-packed-fp32-ops")))
#else
#define NOPK_ATTR
#endif
__global__ __launch_bounds__(NTHR, 2) NOPK_ATTR void s5_fused(const u16* __restrict__ x, const u16* __restrict__ lre, const u16* __restrict__ lim,
    const u16* __restrict__ Bre, const u16* __restrict__ Bim, const u16* __restrict__ Cre, const u16* __restrict__ Cim,
    const u16* __restrict__ logdt, const u16* __restrict__ Dsk, const u16* __restrict__ w1, const u16* __restrict__ b1,
    const u16* __restrict__ w2, const u16* __restrict__ b2, float* __restrict__ out, unsigned char* __restrict__ ws) {
  extern __shared__ __attribute__((aligned(16))) unsigned char smem_[];
  LAS unsigned char* sm = (LAS unsigned char*)smem_;
  asm volatile("" :: "s"(x), "s"(lre), "s"(lim), "s"(Bre), "s"(Bim), "s"(Cre), "s"(Cim), "s"(logdt), "s"(Dsk), "s"(w1), "s"(b1), "s"(w2), "s"(b2), "s"(out), "s"(ws));
  u64 pc0; asm volatile("s_getpc_b64 %0" : "=s"(pc0));
  const int tid = threadIdx.x, lane = tid & 63;
  const int wv = __builtin_amdgcn_readfirstlane(tid >> 6), w = wv & 3;
  const int b = blockIdx.x, xcd = b & 7, idx = b >> 3;
  const int r = 4 * (xcd >> 1) + (idx & 3), jj = idx >> 2, j = 8 * (xcd & 1) + jj;
  const int half2 = jj & 1, ct = 4 * (xcd & 1) + (jj >> 1), rt64 = 2 * r + half2;
  const int s1 = ROT1 == 1 ? (j + 3 * r) % 10 : ROT1 == 2 ? 5 * ((j + r) & 1) : ROT1 == 3 ? 3 * ((j + r) % 3) : 0;
  STAMP(0); TS(0); TG(0);
  const unsigned smb = (unsigned)(size_t)sm;
  const __amdgpu_buffer_rsrc_t rs = __builtin_amdgcn_make_buffer_rsrc((void*)ws, 0, WS_BYTES, 0x00020000);
  LAS unsigned* cnt = (LAS unsigned*)(sm + CNT);
  const int n16 = lane & 15, g = lane >> 4;
  const int s2f = ROT2 ? 3 * (ct & 3) : 0, s2y = ROT2 ? (5 * (ct & 3)) >> 1 : 0;
  const int ctu = b & 7, fu = b >> 3;
  auto read_tag = [&]() __attribute__((always_inline)) {
    const v2u e = *(LAS v2u*)(sm + RAW + 47 * 16);
    return mix64(((u64)__builtin_amdgcn_readfirstlane(e.y) << 32) | (unsigned)__builtin_amdgcn_readfirstlane(e.x));
  };

  if (wv < 4) {
    unsigned so[10];
    { const int xrow = 32 * w + (lane >> 3);
      const unsigned xb = (unsigned)(128 * r + xrow) * 1280u + (((lane & 7) ^ ((xrow >> 1) & 1)) << 4);
#pragma unroll
      for (int q = 0; q < 4; ++q) so[q] = (xb + q * 10240u) ^ (q << 5); }
    const unsigned char* bmat = (const unsigned char*)((w >> 1) ? Bim : Bre);
    const unsigned l16 = lane * 16;
    auto job = [&](int d) __attribute__((always_inline)) {
      if (d < 100) {
        const int st = d / 10, p = d % 10, kt = rotk<10>(st, s1);
        const unsigned sb = smb + (st % 3) * STAGE;
        if (p < 4) dma1((const unsigned char*)x + kt * 128, so[p], sb + XS + (32 * w + 8 * p) * 128);
        else if (p < 6) dma1(bmat + kt * 128, so[p], sb + BWS + 2560 * w + (p - 4) * 1024);
        else if (p == 6) dma1h(bmat + kt * 128, so[p], sb + BWS + 2560 * w + 2048);
        else dma1((const unsigned char*)w1 + (size_t)kt * (64 * 3072), so[p], sb + W1S + 3072 * w + (p - 7) * 1024);
      } else if (d < 102) {
        const int pc = 2 * w + (d - 100);
        dma1s(ws + (pc == 0 ? PRS_OFF + r * 1024 : pc == 1 ? PCB_OFF + j * 1024 : PCF_OFF + j * 6144 + (pc - 2) * 1024), l16,
              smb + (pc == 0 ? ST2 : pc == 1 ? CSB : ESR + (pc - 2) * 1024));
      } else if (d < 127) {
        const int sl = (d - 102) / 5, n = (d - 102) % 5;
        if (sl == 2 || sl == 4) dma1s(ws + WT_OFF + (ct * 88 + 10 * w + rotk<10>(DBY + (sl >> 2), s2y)) * 5120 + n * 1024, l16, smb + yslot(w, sl >> 2) + n * 1024);
        else { const int k = sl == 3 ? 2 : sl; dma1s(ws + WT_OFF + (ct * 88 + 40 + 12 * w + rotk<12>(DBF + k, s2f)) * 5120 + n * 1024, l16, smb + fslot(w, k) + n * 1024); }
      }
    };
#pragma unroll
    for (int d = 0; d < 4; ++d) job(d);
#pragma unroll
    for (int q3 = 0; q3 < 3; ++q3) {
      int ch = 64 * q3 + lane; if (ch > 159) ch = 159;
      const int cl = 20 * (w & 1) + (ch >> 3), cs_ = ch & 7;
      so[4 + q3] = (unsigned)(40 * j + cl) * 1280u + ((cs_ ^ ((cl >> 1) & 7) ^ ((w >> 1) << 2)) << 4);
      const int c2 = 192 * w + 64 * q3 + lane;
      so[7 + q3] = (unsigned)(c2 / 12) * 3072u + j * 192 + (c2 % 12) * 16;
    }
#pragma unroll
    for (int d = 4; d < 10; ++d) job(d);
    if (tid < 16) cnt[tid] = 0u;
    const unsigned o1 = (unsigned)(40 * r + lane / 12) * 3072u + j * 192 + (lane % 12) * 16;
    const unsigned o2 = (unsigned)(48 * fu + 12 * w + lane / 10) * 1280u + ctu * 160 + (lane % 10) * 16;
    const int u = b * 4 + w, cu = u < 800 ? u : u - 800, cd8 = cu / 10, ccb = cu % 10;
    const unsigned o3 = (unsigned)((8 * cd8 + (lane >> 3)) * P + 64 * ccb + 8 * (lane & 7)) * 2;
    auto jobG2 = [&](int q) __attribute__((always_inline)) {
      const int p = 7 * w + q;
      if (p < 25) {
        const unsigned char* gb = p < 10 ? (const unsigned char*)x + (size_t)(128 * r + 8 * j) * 1280 + p * 1024
                                : p < 17 ? (const unsigned char*)(r < 8 ? Bre : Bim) + (size_t)(40 * j + 5 * (r & 7)) * 1280 + (p - 10) * 1024
                                         : (const unsigned char*)w1 + (p - 17) * (5 * 3072);
        dma1m(gb, p < 17 ? l16 : o1, smb + (p < 17 ? XROW + p * 1024 : W1SCR + (p - 17) * 960), p < 16 ? 0 : p == 16 ? 48 : 4);
      } else if (p == 25) {
        const u16* vb = lane < 5 ? lre + 40 * j + 8 * lane : lane < 10 ? lim + 40 * j + 8 * (lane - 5) : lane < 15 ? logdt + 40 * j + 8 * (lane - 10)
                      : lane < 27 ? b1 + 96 * j + 8 * (lane - 15) : lane < 37 ? Dsk + 80 * ct + 8 * (lane - 27) : lane < 47 ? b2 + 80 * ct + 8 * (lane - 37) : (const u16*)(ws + EPOCH_OFF);
        asm volatile("s_mov_b32 m0, %1\n\ts_nop 0\n\tglobal_load_lds_dwordx4 %0, off sc1" :: "v"(vb), "s"(smb + RAW) : "memory");
      }
    };
    auto jobG1 = [&](int q) __attribute__((always_inline)) {
      if (q < 2) dma1m((const unsigned char*)w2 + q * (6 * 1280), o2, smb + W2SCR + (2 * w + q) * 960, 4);
      else dma1(q == 2 ? Cre : Cim, o3, smb + CSCR + w * 2048 + (q - 2) * 1024);
    };
    const int rho = n16;
    const int Rrow = 32 * w + 8 * (rho >> 2) + (rho & 3);
    const int fx = ((rho >> 2) << 1) | ((rho >> 1) & 1);
    const int xa0 = XS + Rrow * 128 + ((g ^ fx) << 4);
    const int wb0 = BWS + rho * 128 + ((g ^ ((rho >> 1) & 7)) << 4);
    const int wb4 = BWS + (40 * (rho & 1) + 32 + (rho >> 1)) * 128 + ((g ^ (rho >> 2) ^ ((rho & 1) << 2)) << 4);
    f32x4 acc[2][5];
#pragma unroll
    for (int i = 0; i < 2; ++i)
#pragma unroll
      for (int t = 0; t < 5; ++t) acc[i][t] = f32x4{0.f, 0.f, 0.f, 0.f};
    v4u fa[2][2][2], fb[2][2][5];
    auto rdB = [&](int set, int st, int q) __attribute__((always_inline)) {
      LAS unsigned char* sb = sm + (st % 3) * STAGE;
      const int kk = q / 7, e = q % 7;
      if (e < 2) fa[set][kk][e] = *(LAS v4u*)(sb + (xa0 ^ (kk * 64)) + e * 512);
      else if (e < 6) { const int t = e - 2; fb[set][kk][t] = *(LAS v4u*)(sb + (wb0 ^ ((kk ^ (t & 1)) * 64)) + (t & 1) * 5120 + (t >> 1) * 2048); }
      else fb[set][kk][4] = *(LAS v4u*)(sb + (wb4 ^ (kk * 64)));
    };
    auto mmB = [&](int set, int m) __attribute__((always_inline)) {
      const int kk = m / 10, t = (m % 10) >> 1, i = m & 1;
      acc[i][t] = mfma16(fa[set][kk][i], fb[set][kk][t], acc[i][t]);
    };
    jobG2(0); jobG2(1);
    waitv<2>();
    BAR();
    STAMP(9);
#pragma unroll
    for (int q = 2; q < 7; ++q) jobG2(q);
#pragma unroll
    for (int q = 0; q < 4; ++q) jobG1(q);
#pragma unroll
    for (int q = 0; q < 14; ++q) rdB(0, 0, q);
    SCHED();
#pragma unroll
    for (int m = 0; m < 20; ++m) { mmB(0, m); if (m & 1) job(10 + (m >> 1)); SCHED(); }
    waitv<10>(); BAR();
#pragma unroll
    for (int d = 20; d < 30; ++d) job(d);
    {
      LAS unsigned char* sc = sm + CSCR + w * 2048 + (lane & 7) * 128 + (lane >> 3) * 8;
      const v2u r0 = *(LAS v2u*)sc, i0 = *(LAS v2u*)(sc + 1024), r1 = *(LAS v2u*)(sc + 64), i1 = *(LAS v2u*)(sc + 1088);
      v4u o0, o1_;
      o0.x = __builtin_amdgcn_perm(i0.x, r0.x, 0x05040100u); o0.y = __builtin_amdgcn_perm(i0.x, r0.x, 0x07060302u);
      o0.z = __builtin_amdgcn_perm(i0.y, r0.y, 0x05040100u); o0.w = __builtin_amdgcn_perm(i0.y, r0.y, 0x07060302u);
      o1_.x = __builtin_amdgcn_perm(i1.x, r1.x, 0x05040100u); o1_.y = __builtin_amdgcn_perm(i1.x, r1.x, 0x07060302u);
      o1_.z = __builtin_amdgcn_perm(i1.y, r1.y, 0x05040100u); o1_.w = __builtin_amdgcn_perm(i1.y, r1.y, 0x07060302u);
      const int ctc = cd8 / 10, ntc = (cd8 % 10) >> 1;
      const int fo = WT_OFF + ((ctc * 88 + 4 * ccb + (lane >> 5)) * 5 + ntc) * 1024 + ((lane >> 3) & 3) * 256 + (8 * (cd8 & 1) + (lane & 7)) * 16;
      __builtin_amdgcn_raw_buffer_store_b128(o0, rs, fo, 0, AUX_SC1);
      __builtin_amdgcn_raw_buffer_store_b128(o1_, rs, fo + 2 * 5120, 0, AUX_SC1);
      SCHED();
    }
#pragma unroll
    for (int d = 30; d < 40; ++d) job(d);
    TS(1);
    waitv<22>(); TS(2); BAR(); TS(3);
#pragma unroll
    for (int q = 0; q < 14; ++q) rdB(1, 1, q);
    WAITL0(); waitv<12>(); BAR();
#pragma unroll
    for (int d = 40; d < 44; ++d) job(d);
    const u64 tag = read_tag();
    v4u efv[2];
    const int boff = WT_OFF + (ct * 88 + 10 * w) * 5120 + lane * 16;
    const int s2 = s2y;
    v4u bw[DBY][5];
    const int q = g, c = n16;
    LAS float* pm = (LAS float*)(sm + PRM);
    f32x4 st0, st1, st2_, st3; float Sc[5]; f32x2 lr2, li2; float lr4, ls4;
    auto stepB = [&](auto I_) __attribute__((always_inline)) {
      constexpr int i = decltype(I_)::value;
      auto jobA = [&](int a) __attribute__((always_inline)) {
        if (i <= 6) { if (a < 6) job(10 * (i + 3) + 4 + a); }
        else if (i == 7) job(106 + a);
        else if (i == 8) job(119 + a);
        else if (a < 5) bw[1][a] = ldsc1(rs, boff + a * 1024, rotk<10>(1, s2) * 5120);
      };
      auto jobB = [&](int a) __attribute__((always_inline)) {
        if (i <= 5) { if (a < 4) job(10 * (i + 4) + a); }
        else if (i == 6) job(100 + a);
        else if (i == 7) job(113 + a);
        else if (i == 8) { if (a == 0) job(126); else bw[0][a - 1] = ldsc1(rs, boff + (a - 1) * 1024, s2 * 5120); }
        else if (a < 5) bw[2][a] = ldsc1(rs, boff + a * 1024, rotk<10>(2, s2) * 5120);
      };
      if (i == 9) {
        LAS f32x4* sp = (LAS f32x4*)(sm + ST2 + (32 * w + 8 * q) * 8);
        st0 = sp[0]; st1 = sp[1]; st2_ = sp[2]; st3 = sp[3];
#pragma unroll
        for (int t = 0; t < 5; ++t) Sc[t] = ((LAS float*)(sm + CSB))[16 * t + c];
        lr2 = f32x2{pm[c], pm[16 + c]}; li2 = f32x2{pm[40 + c], pm[56 + c]};
        lr4 = pm[32 + (c >> 1)]; ls4 = pm[72 + (c >> 1)];
        SCHED();
      }
#pragma unroll
      for (int m = 0; m < 14; ++m) { mmB(i & 1, m); if (i < 9) rdB((i + 1) & 1, i + 1, m); if (m & 1) jobA(m >> 1); SCHED(); }
      if (i < 9) {
        WAITL0();
        TS(1 + 3 * i);
        if (i < 6) waitv<10>();
        else if (i == 6) {
          asm volatile("s_waitcnt vmcnt(10)" : "+v"(efv[0]), "+v"(efv[1]) :: "memory");
          if (w == 1) {
            const unsigned tl = (unsigned)tag, th = (unsigned)(tag >> 32);
            const unsigned bad = (efv[0].x ^ tl) | (efv[0].y ^ th) | (efv[0].z ^ tl) | (efv[0].w ^ th) | (efv[1].x ^ tl) | (efv[1].y ^ th) | (efv[1].z ^ tl) | (efv[1].w ^ th);
            if (!WAVE_ALL(bad == 0u)) {
#pragma nounroll
              for (unsigned spins = 0; spins < SPIN_CAP; ++spins) {
                const v4u e0 = ldsc1(rs, FLAG_E_OFF + xcd * 2048 + lane * 32, 0), e1 = ldsc1(rs, FLAG_E_OFF + xcd * 2048 + lane * 32 + 16, 0);
                if (WAVE_ALL(((e0.x ^ tl) | (e0.y ^ th) | (e0.z ^ tl) | (e0.w ^ th) | (e1.x ^ tl) | (e1.y ^ th) | (e1.z ^ tl) | (e1.w ^ th)) == 0u)) break;
                asm volatile("s_sleep 2" ::: "memory");
              }
            }
            if (b == 0 && lane == 0) __hip_atomic_store((GAS u64*)(ws + EPOCH_OFF), tag, RLX_AGENT);
          }
        }
        else if (i == 7) waitv<13>();
        else waitv<24>();
        TS(2 + 3 * i);
        BAR();
        TS(3 + 3 * i);
      }
      if (i == 2 && tid < 8) __hip_atomic_store((GAS u64*)(ws + FLAG_E_OFF) + tid * 256 + b, tag, RLX_AGENT);
      if (i == 5) {
        asm volatile("s_cmp_eq_u32 %4, 1\n\ts_cselect_b64 exec, -1, 0\n\tglobal_load_dwordx4 %0, %2, %3 sc1\n\tglobal_load_dwordx4 %1, %2, %3 offset:16 sc1\n\ts_mov_b64 exec, -1"
                     : "=&v"(efv[0]), "=&v"(efv[1]) : "v"(l16 * 2), "s"(ws + FLAG_E_OFF + xcd * 2048), "s"(w) : "memory", "scc");
      }
      if (i == 8 && w == 3) {
        const int l48 = lane < 48 ? lane : 47;
        f32x2 a = *(LAS f32x2*)(sm + ESR + l48 * 8);
#pragma unroll
        for (int k = 1; k < 16; ++k) a += *(LAS f32x2*)(sm + ESR + k * 384 + l48 * 8);
        *(LAS f32x2*)(sm + CSF + l48 * 8) = a;
        lds_signal(cnt + 8, lane);
      }
      SCHED();
#pragma unroll
      for (int m = 14; m < 20; ++m) { mmB(i & 1, m); jobB(m - 14); SCHED(); }
    };
    stepB(IC(1)); stepB(IC(2)); stepB(IC(3)); stepB(IC(4)); stepB(IC(5)); stepB(IC(6)); stepB(IC(7)); stepB(IC(8)); stepB(IC(9));
    STAMP(1); TQ(0);
    __builtin_amdgcn_s_setprio(2);
    f32x2 ur[8], ui[8], er, ei;
    float u4[8], e4;
    ls4 = (c & 1) ? ls4 : -ls4;
    {
      const float rsa[4] = {st0[0], st0[2], st1[0], st1[2]}, rsb[4] = {st2_[0], st2_[2], st3[0], st3[2]};
      f32x2 mba[4] = {{st0[1], st0[1]}, {st0[3], st0[3]}, {st1[1], st1[1]}, {st1[3], st1[3]}}, mbb[4] = {{st2_[1], st2_[1]}, {st2_[3], st2_[3]}, {st3[1], st3[1]}, {st3[3], st3[3]}};
#pragma unroll
      for (int s = 0; s < 4; ++s) { asm volatile("" : "+v"(mba[s])); asm volatile("" : "+v"(mbb[s])); }
      const f32x2 Sr = {Sc[0], Sc[2]}, Si = {Sc[1], Sc[3]};
#pragma unroll
      for (int s = 0; s < 4; ++s) {
        ur[s] = (f32x2{acc[0][0][s], acc[0][2][s]} - mba[s] * Sr) * rsa[s]; ui[s] = (f32x2{acc[0][1][s], acc[0][3][s]} - mba[s] * Si) * rsa[s];
        ur[4 + s] = (f32x2{acc[1][0][s], acc[1][2][s]} - mbb[s] * Sr) * rsb[s]; ui[4 + s] = (f32x2{acc[1][1][s], acc[1][3][s]} - mbb[s] * Si) * rsb[s];
        u4[s] = (acc[0][4][s] - mba[s][0] * Sc[4]) * rsa[s]; u4[4 + s] = (acc[1][4][s] - mbb[s][0] * Sc[4]) * rsb[s];
      }
      er = ur[0]; ei = ui[0]; e4 = u4[0];
#pragma unroll
      for (int s = 1; s < 8; ++s) {
        const f32x2 nr = lr2 * er + (ur[s] - li2 * ei), ni = lr2 * ei + (li2 * er + ui[s]);
        er = nr; ei = ni;
        e4 = lr4 * e4 + (ls4 * swp1(e4) + u4[s]);
      }
    }
    lds_wait(cnt + 8, 1u);
    LAS float* es = (LAS float*)(sm + ESR);
    es[(4 * w + q) * 80 + 2 * c] = er[0]; es[(4 * w + q) * 80 + 2 * c + 1] = ei[0];
    es[(4 * w + q) * 80 + 32 + 2 * c] = er[1]; es[(4 * w + q) * 80 + 33 + 2 * c] = ei[1];
    es[(4 * w + q) * 80 + 64 + c] = e4;
    lds_signal(cnt + 0, lane);
    TQ(1);
    const int l40 = lane < 40 ? lane : 39;
    const unsigned tlo = (unsigned)tag >> 24 << 24, thi = (unsigned)(tag >> 32);
    if (w == 0) {
      const float l8r = pm[80 + l40], l8i = pm[120 + l40];
      lds_wait(cnt + 0, 4u);
      TQ(2);
      f32x2 ev[16];
#pragma unroll
      for (int s = 0; s < 16; ++s) ev[s] = *(LAS f32x2*)(es + s * 80 + 2 * l40);
      float Er = 0.f, Ei = 0.f;
#pragma unroll
      for (int s = 0; s < 16; ++s) {
        const float pr_ = Er, pi_ = Ei;
        Er = __builtin_fmaf(l8r, pr_, __builtin_fmaf(-l8i, pi_, ev[s][0])); Ei = __builtin_fmaf(l8r, pi_, __builtin_fmaf(l8i, pr_, ev[s][1]));
        asm volatile("" : "+v"(Er)); asm volatile("" : "+v"(Ei));
        ev[s] = f32x2{pr_, pi_};
      }
      if (r < 15 && lane < 40) {
        v4u o; o.x = tlo | ((__builtin_bit_cast(unsigned, Er) + 0x80u) >> 8); o.y = thi; o.z = tlo | ((__builtin_bit_cast(unsigned, Ei) + 0x80u) >> 8); o.w = thi;
        __builtin_amdgcn_raw_buffer_store_b128(o, rs, EG_OFF + ((j * 16 + r) * 40 + lane) * 16, 0, AUX_SC1);
      }
      STAMP(2); TQ(3);
#pragma unroll
      for (int s = 0; s < 16; ++s) *(LAS f32x2*)(es + s * 80 + 2 * l40) = ev[s];
    }
    waitv<15>();
    if (lane == 0) __hip_atomic_fetch_add(cnt + 4 + w, 1u, RLX_WG);
#pragma unroll
    for (int k = PBY; k < DBY; ++k)
#pragma unroll
      for (int n = 0; n < 5; ++n) bw[k][n] = ldsc1(rs, boff + n * 1024, rotk<10>(k, s2) * 5120);
    SCHED();
    f32x2 gr = {1.f, 1.f}, gi = {0.f, 0.f}, g4r = {1.f, 1.f}, g4i = {0.f, 0.f};
    { f32x2 pr = {pm[80 + c], pm[96 + c]}, pi = {pm[120 + c], pm[136 + c]};
      f32x2 qr = {pm[112 + (c >> 1)], 0.f}, qi = {pm[152 + (c >> 1)], 0.f};
      const int sg = 4 * w + q;
#pragma unroll
      for (int bt = 0; bt < 4; ++bt) {
        const f32x2 tr_ = gr * pr - gi * pi, ti_ = gr * pi + gi * pr, ur_ = g4r * qr - g4i * qi, ui_ = g4r * qi + g4i * qr;
        if ((sg >> bt) & 1) { gr = tr_; gi = ti_; g4r = ur_; g4i = ui_; }
        if (bt < 3) { const f32x2 n1 = pr * pr - pi * pi, n2 = qr * qr - qi * qi; pi = 2.f * pr * pi; pr = n1; qi = 2.f * qr * qi; qr = n2; }
      } }
    const float g4s = (c & 1) ? g4i[0] : -g4i[0];
    const f32x2 cr2 = f32x2{2.f * pm[240 + c], 2.f * pm[256 + c]}, ci2 = f32x2{2.f * pm[280 + c], 2.f * pm[296 + c]};
    const float cr4 = pm[272 + (c >> 1)], ca4 = (c & 1) ? -2.f * cr4 : 2.f * cr4, cb4 = -2.f * pm[312 + (c >> 1)];
    int hoffs[5];
#pragma unroll
    for (int m = 0; m < 5; ++m) {
      const int unit = 4 * m + g, kap = 10 * j + (unit >> 1);
      hoffs[m] = HT_OFF + (((2 * r + (w >> 1)) * 40 + (kap >> 2)) * 4 + 2 * (w & 1) + (unit & 1)) * 1024 + (kap & 3) * 256 + n16 * 16;
      asm volatile("" : "+v"(hoffs[m]));
    }
    TQ(4);
    if (w < 2) {
      float Hr_ = 0.f, Hi_ = 0.f;
      if (r > 8 * w) {
        const float lKr = pm[160 + l40], lKi = pm[200 + l40];
        float p4r, p4i, p8r, p8i;
        { const float a2r = lKr * lKr - lKi * lKi, a2i = 2.f * lKr * lKi; p4r = a2r * a2r - a2i * a2i; p4i = 2.f * a2r * a2i; p8r = p4r * p4r - p4i * p4i; p8i = 2.f * p4r * p4i; }
        const int eo = EG_OFF + (j * 16 * 40 + l40) * 16, top = r - 1 - 8 * w;
        v4u gv[8];
#pragma nounroll
        for (unsigned spins = 0; spins < SPIN_CAP; ++spins) {
#pragma unroll
          for (int m = 0; m < 8; ++m) gv[m] = ldsc1(rs, eo, (m <= top ? top - m : 0) * 640);
          unsigned bad = 0u, bad2 = 0u;
#pragma unroll
          for (int m = 0; m < 8; ++m) { bad |= (gv[m].x ^ tlo) | (gv[m].z ^ tlo); bad2 |= (gv[m].y ^ thi) | (gv[m].w ^ thi); }
          if (WAVE_ALL(((bad >> 24) | bad2) == 0u)) break;
          asm volatile("s_sleep 1" ::: "memory");
        }
        float hr_[2] = {0.f, 0.f}, hi_[2] = {0.f, 0.f};
#pragma unroll
        for (int m = 3; m >= 0; --m)
#pragma unroll
          for (int k = 0; k < 2; ++k) {
            const bool ok = 4 * k + m <= top;
            const float er_ = ok ? __builtin_bit_cast(float, gv[4 * k + m].x << 8) : 0.f, ei_ = ok ? __builtin_bit_cast(float, gv[4 * k + m].z << 8) : 0.f;
            const float nr_ = __builtin_fmaf(lKr, hr_[k], __builtin_fmaf(-lKi, hi_[k], er_)), ni_ = __builtin_fmaf(lKr, hi_[k], __builtin_fmaf(lKi, hr_[k], ei_));
            hr_[k] = nr_; hi_[k] = ni_;
            asm volatile("" : "+v"(hr_[k])); asm volatile("" : "+v"(hi_[k]));
          }
        Hr_ = p4r * hr_[1] - p4i * hi_[1] + hr_[0]; Hi_ = p4r * hi_[1] + p4i * hr_[1] + hi_[0];
        asm volatile("" : "+v"(Hr_)); asm volatile("" : "+v"(Hi_));
        if (w == 1) { const float t_ = p8r * Hr_ - p8i * Hi_; Hi_ = p8r * Hi_ + p8i * Hr_; Hr_ = t_; asm volatile("" : "+v"(Hr_)); asm volatile("" : "+v"(Hi_)); }
      }
      STAMP(3);
      ((LAS float*)(sm + HIN))[80 * w + 2 * l40] = Hr_; ((LAS float*)(sm + HIN))[80 * w + 2 * l40 + 1] = Hi_;
      lds_signal(cnt + 1, lane);
    }
    TQ(5);
    {
      lds_wait(cnt + 1, 2u);
      TQ(6);
      LAS unsigned char* scr = sm + w * 10240;
      LAS float* hin = (LAS float*)(sm + HIN);
      const f32x2 Hr = {hin[2 * c] + hin[80 + 2 * c], hin[32 + 2 * c] + hin[112 + 2 * c]}, Hi = {hin[2 * c + 1] + hin[81 + 2 * c], hin[33 + 2 * c] + hin[113 + 2 * c]};
      f32x2 hr = {es[(4 * w + q) * 80 + 2 * c], es[(4 * w + q) * 80 + 32 + 2 * c]}, hi = {es[(4 * w + q) * 80 + 2 * c + 1], es[(4 * w + q) * 80 + 33 + 2 * c]};
      hr += gr * Hr - gi * Hi; hi += gr * Hi + gi * Hr;
      float h4 = es[(4 * w + q) * 80 + 64 + c] + (g4r[0] * (hin[64 + c] + hin[144 + c]) + g4s * (hin[64 + (c ^ 1)] + hin[144 + (c ^ 1)])), hp4 = swp1(h4);
      f32x2 o2[2][8]; float o4[8];
#pragma unroll
      for (int s = 0; s < 8; ++s) {
        const f32x2 nr = lr2 * hr + (ur[s] - li2 * hi), ni = lr2 * hi + (li2 * hr + ui[s]);
        hr = nr; hi = ni;
        o2[0][s] = cr2 * hr - ci2 * hi;
        o2[1][s] = -(cr2 * hi + ci2 * hr);
        h4 = lr4 * h4 + (ls4 * hp4 + u4[s]); hp4 = swp1(h4);
        o4[s] = ca4 * h4 + cb4 * hp4;
      }
#pragma unroll
      for (int p = 0; p < 2; ++p)
#pragma unroll
        for (int pt = 0; pt < 2; ++pt) {
          v4u ov; ov.x = pk2(o2[pt][0][p], o2[pt][1][p]); ov.y = pk2(o2[pt][2][p], o2[pt][3][p]); ov.z = pk2(o2[pt][4][p], o2[pt][5][p]); ov.w = pk2(o2[pt][6][p], o2[pt][7][p]);
          *(LAS v4u*)(scr + (32 * p + 2 * c + pt) * 80 + q * 16) = ov;
        }
      { v4u ov; ov.x = pk2(o4[0], o4[1]); ov.y = pk2(o4[2], o4[3]); ov.z = pk2(o4[4], o4[5]); ov.w = pk2(o4[6], o4[7]);
        *(LAS v4u*)(scr + (64 + c) * 80 + q * 16) = ov; }
      WAITL0();
      TQ(7);
      LAS unsigned char* ra = scr + (8 * (g >> 1) + ((lane & 15) >> 2)) * 80 + (16 * (g & 1) + 4 * (lane & 3)) * 2;
      v2u lo[5], hi_[5];
#pragma unroll
      for (int m = 0; m < 5; ++m) { lo[m] = trread(ra + m * 1280); hi_[m] = trread(ra + m * 1280 + 320); }
#pragma unroll
      for (int m = 0; m < 5; ++m) {
        v4u ov; ov.x = lo[m].x; ov.y = lo[m].y; ov.z = hi_[m].x; ov.w = hi_[m].y;
        __builtin_amdgcn_raw_buffer_store_b128(ov, rs, hoffs[m], 0, AUX_SC1);
      }
    }
    STAMP(4); TQ(8);
    SCHED();
    waitv<0>();
    if (lane == 0) __hip_atomic_store((GAS u64*)(ws + FLAG_H_OFF) + ((r * 2 + (w >> 1)) * 16 + j) * 2 + (w & 1), tag, RLX_AGENT);
    STAMP(5); TQ(9);
    __builtin_amdgcn_s_setprio(0);
    TQ(10);
    poll_flags((const GAS u64*)(ws + FLAG_H_OFF) + ((r * 2 + half2) * 16 + 4 * w) * 2 + (lane & 7), tag);
    STAMP(6); TQ(11); TG(1);
    f32x4 ac2[5][4];
#pragma unroll
    for (int n = 0; n < 5; ++n)
#pragma unroll
      for (int i = 0; i < 4; ++i) ac2[n][i] = f32x4{0.f, 0.f, 0.f, 0.f};
    int aoff[4];
#pragma unroll
    for (int i = 0; i < 4; ++i) aoff[i] = HT_OFF + ((rt64 * 40 + 10 * w) * 4 + ((i + w) & 3)) * 1024 + lane * 16;
    const size_t orow = (size_t)(128 * r + 64 * half2 + 16 * w + n16) * D + 80 * ct + 4 * g;
    v2u xv[5];
    stream_gemm<10, DBY, DAY, NLY>(rs, boff, aoff, s2, bw, ac2,
        [&](int k) __attribute__((always_inline)) { return sm + yslot(w, k) + lane * 16; },
        [&]() __attribute__((always_inline)) {
#pragma unroll
          for (int n = 0; n < 5; ++n) xv[n] = *(const v2u*)(x + orow + 16 * n);
        }, [&](int k) __attribute__((always_inline)) { TG(2 + k); });
    STAMP(7); TQ(12);
    auto ypart = [&](int ww, int i) __attribute__((always_inline)) { return sm + (i < 2 ? ww * 10240 + i * 5120 : yslot(ww, 0)) + lane * 16; };
#pragma unroll
    for (int i = 1; i < 4; ++i)
#pragma unroll
      for (int n = 0; n < 5; ++n) *(LAS f32x4*)(ypart(w, i - 1) + n * 1024) = ac2[n][i];
    lds_signal(cnt + 3, lane);
    TQ(13);
    lds_wait(cnt + 10 + w, 1u);
    v4u gs[5];
#pragma unroll
    for (int n = 0; n < 5; ++n) {
      const v2u gq = *(LAS v2u*)(sm + fslot(w, 0) + n * 512 + lane * 8), sq = *(LAS v2u*)(sm + SKR + w * 2560 + n * 512 + lane * 8);
      gs[n] = v4u{gq.x, gq.y, sq.x, sq.y};
    }
    TQ(14);
    lds_wait(cnt + 3, 4u);
    TQ(15);
#pragma unroll
    for (int n = 0; n < 5; ++n) {
      f32x4 y = ac2[n][0] + f32x4{bf2f(gs[n].z & 0xffffu), bfhi(gs[n].z), bf2f(gs[n].w & 0xffffu), bfhi(gs[n].w)};
#pragma unroll
      for (int o = 1; o < 4; ++o) y += *(LAS f32x4*)(ypart((w + o) & 3, 3 - o) + n * 1024);
      const f32x4 gg = {bf2f(gs[n].x & 0xffffu), bfhi(gs[n].x), bf2f(gs[n].y & 0xffffu), bfhi(gs[n].y)};
      const f32x4 xx = {bf2f(xv[n].x & 0xffffu), bfhi(xv[n].x), bf2f(xv[n].y & 0xffffu), bfhi(xv[n].y)};
      __builtin_nontemporal_store(xx + y * gg, (f32x4*)(out + orow + 16 * n));
    }
    STAMP(8); TQ(16);
    TS_OUT();
  } else {
    {
      const int line = (idx * 4 + w) * 3 + (lane < 3 ? lane : 2);
      if (line < CODE_LINES)
        asm volatile("s_mov_b32 m0, %2\n\ts_nop 0\n\tglobal_load_lds_dword %1, %0" :: "s"(pc0), "v"(line * 128), "s"(smb + JUNK) : "memory");
    }
    const int t32 = lane & 31, hh = lane >> 5;
    const int fx = (((t32 >> 3) & 3) << 1) | ((t32 >> 1) & 1);
    const int xa0 = XS + (32 * w + t32) * 128 + ((hh ^ fx) << 4);
    const int wa0 = W1S + (8 * hh + ((lane & 15) >> 2)) * 192 + (16 * ((lane >> 4) & 1) + 4 * (lane & 3)) * 2;
    f32x16 acc[3];
#pragma unroll
    for (int n = 0; n < 3; ++n)
#pragma unroll
      for (int e = 0; e < 16; ++e) acc[n][e] = 0.f;
    v4u xf[2][4]; v2u wf[2][4][3][2];
    auto rdF = [&](int set, int st, int q) __attribute__((always_inline)) {
      LAS unsigned char* sb = sm + (st % 3) * STAGE;
      const int s = q / 7, e = q % 7;
      if (e == 0) xf[set][s] = *(LAS v4u*)(sb + (xa0 ^ (s * 32)));
      else wf[set][s][(e - 1) >> 1][(e - 1) & 1] = trread(sb + wa0 + s * 3072 + ((e - 1) & 1) * 768 + ((e - 1) >> 1) * 64);
    };
    auto wfrag = [&](int set, int s, int n) __attribute__((always_inline)) { v4u v; v.x = wf[set][s][n][0].x; v.y = wf[set][s][n][0].y; v.z = wf[set][s][n][1].x; v.w = wf[set][s][n][1].y; return v; };
    auto mmF = [&](int set, int m) __attribute__((always_inline)) { const int s = m / 3, n = m % 3; acc[n] = mfma32(wfrag(set, s, n), xf[set][s], acc[n]); };
    BAR();
    STAMP(9);
#pragma unroll
    for (int q = 0; q < 28; ++q) rdF(0, 0, q);
    SCHED();
#pragma unroll
    for (int m = 0; m < 12; ++m) { mmF(0, m); SCHED(); }
    BAR();
    const unsigned ones = 0x3f803f80u;
    {
      {
        LAS unsigned char* xp = sm + XROW + (2 * w + hh) * 1280;
        const v4u xa = *(LAS v4u*)(xp + t32 * 16), xb = *(LAS v4u*)(xp + 512 + t32 * 16); const v2u xc = *(LAS v2u*)(xp + 1024 + t32 * 8);
        const unsigned xw[10] = {xa.x, xa.y, xa.z, xa.w, xb.x, xb.y, xb.z, xb.w, xc.x, xc.y};
        f32x2 xv2[10], a2 = {0.f, 0.f};
#pragma unroll
        for (int k = 0; k < 10; ++k) { xv2[k] = f32x2{bf2f(xw[k] & 0xffffu), bfhi(xw[k])}; a2 += xv2[k]; }
        const float mean = sum32(a2[0] + a2[1]) * (1.f / D);
        f32x2 q2 = {0.f, 0.f};
#pragma unroll
        for (int k = 0; k < 10; ++k) { const f32x2 d0 = xv2[k] - mean; q2 += d0 * d0; }
        const float var = sum32(q2[0] + q2[1]) * (1.f / D);
        const f32x2 st = {__builtin_amdgcn_rcpf(rbf(__builtin_amdgcn_sqrtf(rbf(rbf(var) + 1e-5f)))), rbf(mean)};
        if (t32 == 0) __builtin_amdgcn_raw_buffer_store_b64(__builtin_bit_cast(v2u, st), rs, PRS_OFF + (r * 128 + 8 * j + 2 * w + hh) * 8, 0, AUX_SC1);
        SCHED();
      }
      {
        if (w < 3) {
          const int br = 2 * w + hh > 4 ? 4 : 2 * w + hh;
          LAS unsigned char* xp = sm + BROW + br * 1280;
          const v4u xa = *(LAS v4u*)(xp + t32 * 16), xb = *(LAS v4u*)(xp + 512 + t32 * 16); const v2u xc = *(LAS v2u*)(xp + 1024 + t32 * 8);
          const int gl = (lane >> 4) & 1, f16 = 3 * fu + w;
          v2u lo[3], hi[3];
#pragma unroll
          for (int q3 = 0; q3 < 3; ++q3) {
            const int nt = q3 < 2 ? 2 * q3 + hh : 4;
            LAS unsigned char* ra = sm + W2SCR + (16 * w + 4 * gl + ((lane & 15) >> 2)) * 160 + (16 * nt + 4 * (lane & 3)) * 2;
            lo[q3] = trread(ra); hi[q3] = trread(ra + 8 * 160);
          }
          const float c1 = sum32(dot2(xc.y, ones, dot2(xc.x, ones, sum8(xb, sum8(xa, 0.f)))));
          const int chn = 5 * (r & 7) + br, part = r >> 3;
          const int col = chn < 32 ? 32 * (chn >> 4) + 16 * part + (chn & 15) : 2 * chn + part;
          if (t32 == 0) __builtin_amdgcn_raw_buffer_store_b32(__builtin_bit_cast(unsigned, c1), rs, PCB_OFF + (j * 256 + col) * 4, 0, AUX_SC1);
#pragma unroll
          for (int q3 = 0; q3 < 3; ++q3) {
            const int nt = q3 < 2 ? 2 * q3 + hh : 4;
            v4u ov; ov.x = lo[q3].x; ov.y = lo[q3].y; ov.z = hi[q3].x; ov.w = hi[q3].y;
            __builtin_amdgcn_raw_buffer_store_b128(ov, rs, WT_OFF + ((ctu * 88 + 40 + (f16 >> 1)) * 5 + nt) * 1024 + (2 * (f16 & 1) + gl) * 256 + n16 * 16, 0, AUX_SC1);
          }
        } else {
          float cs6[6] = {0.f, 0.f, 0.f, 0.f, 0.f, 0.f};
#pragma unroll
          for (int m = 0; m < 3; ++m) {
            const int kq = m < 2 ? g + 4 * m : 8 + (g & 1);
            const unsigned wgt = (m < 2 || g < 2) ? ones : 0u;
            LAS unsigned char* ra = sm + W1SCR + (4 * kq + ((lane & 15) >> 2)) * 192 + 8 * (lane & 3);
#pragma unroll
            for (int c6 = 0; c6 < 6; ++c6) { const v2u v = trread(ra + c6 * 32); cs6[c6] = dot2(v.y, wgt, dot2(v.x, wgt, cs6[c6])); }
          }
#pragma unroll
          for (int c6 = 0; c6 < 6; ++c6) cs6[c6] = add32(add16(cs6[c6]));
          const float v0 = g == 0 ? cs6[0] : g == 1 ? cs6[1] : g == 2 ? cs6[2] : cs6[3], v1 = (g & 1) ? cs6[5] : cs6[4];
          const int po = PCF_OFF + (j * 16 + r) * 384;
          __builtin_amdgcn_raw_buffer_store_b32(__builtin_bit_cast(unsigned, v0), rs, po + lane * 4, 0, AUX_SC1);
          __builtin_amdgcn_raw_buffer_store_b32(__builtin_bit_cast(unsigned, v1), rs, po + 256 + t32 * 4, 0, AUX_SC1);
        }
        SCHED();
      }
      {
        LAS u16* raw = (LAS u16*)(sm + RAW);
        if (w == 0) {
          const int l40 = lane < 40 ? lane : 39;
          const float dt = rbf(__expf(bf2f(raw[80 + l40])));
          const float lr = bf2f(raw[l40]), li = bf2f(raw[40 + l40]);
          const float mag = __expf(dt * lr);
          float rev = dt * li * 0.15915494309189535f; rev -= floorf(rev);
          const float ar = mag * __builtin_amdgcn_cosf(rev), ai = mag * __builtin_amdgcn_sinf(rev);
          const float nr = ar - 1.f, den = __builtin_amdgcn_rcpf(lr * lr + li * li);
          const float cr = (nr * lr + ai * li) * den, ci = (ai * lr - nr * li) * den;
          float pr = ar, pi = ai;
#pragma unroll
          for (int s = 0; s < 3; ++s) { const float t = pr * pr - pi * pi; pi = 2.f * pr * pi; pr = t; }
          const float p8r = pr, p8i = pi;
#pragma unroll
          for (int s = 0; s < 4; ++s) { const float t = pr * pr - pi * pi; pi = 2.f * pr * pi; pr = t; }
          LAS float* pm = (LAS float*)(sm + PRM) + l40;
          pm[0] = ar; pm[40] = ai; pm[80] = p8r; pm[120] = p8i; pm[160] = pr; pm[200] = pi; pm[240] = cr; pm[280] = ci;
        } else if (w == 1) {
          const int l48 = lane < 48 ? lane : 47;
          const unsigned v = *(LAS unsigned*)(sm + RAW + 240 + l48 * 4);
          *(LAS f32x2*)(sm + B1F + l48 * 8) = f32x2{bf2f(v & 0xffffu), bfhi(v)};
        } else {
          const int l40 = lane < 40 ? lane : 39;
          const unsigned v = *(LAS unsigned*)(sm + RAW + 432 + (w - 2) * 160 + l40 * 4);
          *(LAS f32x2*)(sm + DB2 + (w - 2) * 320 + l40 * 8) = f32x2{bf2f(v & 0xffffu), bfhi(v)};
        }
        SCHED();
      }
    }
    TS(1); TS(2); BAR(); TS(3);
#pragma unroll
    for (int q = 0; q < 28; ++q) rdF(1, 1, q);
    WAITL0(); BAR();
    const u64 tag = read_tag();
    v4u bw[DBF][5];
    const int boff = WT_OFF + (ct * 88 + 40 + 12 * w) * 5120 + lane * 16;
    const int s2 = s2f;
    auto stepF = [&](auto I_) __attribute__((always_inline)) {
      constexpr int i = decltype(I_)::value;
#pragma unroll
      for (int m = 0; m < 8; ++m) {
        mmF(i & 1, m);
        if (i < 9) {
#pragma unroll
          for (int q = (m < 4 ? 4 * m : 3 * m + 4); q < (m < 4 ? 4 * m + 4 : 3 * m + 7); ++q) rdF((i + 1) & 1, i + 1, q);
        }
        SCHED();
      }
      if (i < 9) {
        WAITL0();
        TS(1 + 3 * i);
        if (i == 2) waitv<0>();
        TS(2 + 3 * i);
        BAR();
        TS(3 + 3 * i);
      }
      SCHED();
#pragma unroll
      for (int m = 8; m < 12; ++m) {
        mmF(i & 1, m);
        if constexpr (i >= 8) {
          bw[i - 8][m - 8] = ldsc1(rs, boff + (m - 8) * 1024, rotk<12>(i - 8, s2) * 5120);
          if (m == 11) bw[i - 8][4] = ldsc1(rs, boff + 4096, rotk<12>(i - 8, s2) * 5120);
        }
        SCHED();
      }
    };
    stepF(IC(1)); stepF(IC(2)); stepF(IC(3)); stepF(IC(4)); stepF(IC(5)); stepF(IC(6)); stepF(IC(7)); stepF(IC(8)); stepF(IC(9));
    STAMP(1); TQ(0);
    {
      lds_wait(cnt + 8, 1u);
      const f32x2 st2 = *(LAS f32x2*)(sm + ST2 + (32 * w + t32) * 8);
      f32x4 S4[12], b4[12];
#pragma unroll
      for (int k = 0; k < 12; ++k) { S4[k] = *(LAS f32x4*)(sm + CSF + (8 * k + 4 * hh) * 4); b4[k] = *(LAS f32x4*)(sm + B1F + (8 * k + 4 * hh) * 4); }
      const float rsd = st2[0], nm = -rsd * st2[1];
      const int hoff = H1T_OFF + (((2 * r + (w >> 1)) * 48 + 3 * j) * 4 + 2 * (w & 1) + (t32 >> 4)) * 1024 + hh * 256 + (t32 & 15) * 16;
#pragma unroll
      for (int n = 0; n < 3; ++n) {
        unsigned ou[8];
#pragma unroll
        for (int e = 0; e < 8; ++e) {
          const f32x2 a = {acc[n][2 * e], acc[n][2 * e + 1]};
          const f32x4 S = S4[4 * n + (e >> 1)], bb = b4[4 * n + (e >> 1)];
          const f32x2 S2 = {S[2 * (e & 1)], S[2 * (e & 1) + 1]}, b2_ = {bb[2 * (e & 1)], bb[2 * (e & 1) + 1]};
          const f32x2 v = rsd * a + (nm * S2 + b2_);
          const f32x2 z = v * (-2.302208198f - 0.1029432397f * (v * v));
          f32x2 d = {__builtin_amdgcn_exp2f(z[0]), __builtin_amdgcn_exp2f(z[1])};
          d += 1.f;
          const f32x2 rc = {__builtin_amdgcn_rcpf(d[0]), __builtin_amdgcn_rcpf(d[1])};
          const f32x2 gl = v * rc;
          ou[e] = pk2(gl[0], gl[1]);
        }
        __builtin_amdgcn_raw_buffer_store_b128(v4u{ou[0], ou[1], ou[2], ou[3]}, rs, hoff + n * 4096, 0, AUX_SC1);
        __builtin_amdgcn_raw_buffer_store_b128(v4u{ou[4], ou[5], ou[6], ou[7]}, rs, hoff + n * 4096 + 512, 0, AUX_SC1);
        SCHED();
        TQ(1 + n);
      }
    }
    STAMP(4);
    v2u xv[5];
    { const u16* xp = x + (size_t)(128 * r + 64 * half2 + 16 * w + n16) * D + 80 * ct + 4 * g;
#pragma unroll
      for (int n = 0; n < 5; ++n) xv[n] = *(const v2u*)(xp + 16 * n); }
    SCHED();
    waitv<5>();
    if (lane == 0) __hip_atomic_store((GAS u64*)(ws + FLAG_F_OFF) + ((r * 2 + (w >> 1)) * 16 + j) * 2 + (w & 1), tag, RLX_AGENT);
    STAMP(5); TQ(4);
#pragma unroll
    for (int k = PBF; k < DBF; ++k)
#pragma unroll
      for (int n = 0; n < 5; ++n) bw[k][n] = ldsc1(rs, boff + n * 1024, rotk<12>(k, s2) * 5120);
    const int trow = 64 * half2 + 16 * w + n16;
    {
      float est[2] = {((LAS float*)(sm + ST2))[2 * trow], ((LAS float*)(sm + ST2))[2 * trow + 1]};
      asm volatile("" : "+v"(est[0])); asm volatile("" : "+v"(est[1]));
#pragma unroll
      for (int n = 0; n < 5; ++n) {
        const f32x4 ds = *(LAS f32x4*)(sm + DB2 + (16 * n + 4 * g) * 4);
        const unsigned d0 = pk2(bf2f(xv[n].x & 0xffffu) - est[1], bfhi(xv[n].x) - est[1]), d1 = pk2(bf2f(xv[n].y & 0xffffu) - est[1], bfhi(xv[n].y) - est[1]);
        const unsigned n0 = pk2(bf2f(d0 & 0xffffu) * est[0], bfhi(d0) * est[0]), n1 = pk2(bf2f(d1 & 0xffffu) * est[0], bfhi(d1) * est[0]);
        *(LAS v2u*)(sm + SKR + w * 2560 + n * 512 + lane * 8) = v2u{pk2(bf2f(n0 & 0xffffu) * ds[0], bfhi(n0) * ds[1]), pk2(bf2f(n1 & 0xffffu) * ds[2], bfhi(n1) * ds[3])};
      }
    }
    TQ(5);
    poll_flags((const GAS u64*)(ws + FLAG_F_OFF) + ((r * 2 + half2) * 16 + 4 * w) * 2 + (lane & 7), tag);
    STAMP(6); TQ(6); TG(1);
    f32x4 ac2[5][4];
#pragma unroll
    for (int n = 0; n < 5; ++n)
#pragma unroll
      for (int i = 0; i < 4; ++i) ac2[n][i] = f32x4{0.f, 0.f, 0.f, 0.f};
    int aoff[4];
#pragma unroll
    for (int i = 0; i < 4; ++i) aoff[i] = H1T_OFF + ((rt64 * 48 + 12 * w) * 4 + ((i + w) & 3)) * 1024 + lane * 16;
    __builtin_amdgcn_s_setprio(1);
    stream_gemm<12, DBF, DAF, NLF>(rs, boff, aoff, s2, bw, ac2,
        [&](int k) __attribute__((always_inline)) { if (k == 0) lds_wait(cnt + 4 + w, 1u); return sm + fslot(w, k) + lane * 16; },
        [&]() __attribute__((always_inline)) {}, [&](int k) __attribute__((always_inline)) { TG(2 + k); });
    STAMP(7); TQ(7);
#pragma unroll
    for (int i = 1; i < 4; ++i)
#pragma unroll
      for (int n = 0; n < 5; ++n) *(LAS f32x4*)(sm + fslot(w, i - 1) + n * 1024 + lane * 16) = ac2[n][i];
    lds_signal(cnt + 2, lane);
    v2u gs[5];
    TQ(8);
    lds_wait(cnt + 2, 4u);
    TQ(9);
    f32x4 h2[5];
#pragma unroll
    for (int n = 0; n < 5; ++n) {
      h2[n] = ac2[n][0];
#pragma unroll
      for (int o = 1; o < 4; ++o) h2[n] += *(LAS f32x4*)(sm + fslot((w + o) & 3, 3 - o) + n * 1024 + lane * 16);
    }
    lds_signal(cnt + 9, lane);
#pragma unroll
    for (int n = 0; n < 5; ++n) {
      const f32x4 bs = *(LAS f32x4*)(sm + DB2 + 320 + (16 * n + 4 * g) * 4);
      const unsigned r0 = pk2(h2[n][0], h2[n][1]), r1 = pk2(h2[n][2], h2[n][3]);
      const unsigned a0 = pk2(bf2f(r0 & 0xffffu) + bs[0], bfhi(r0) + bs[1]), a1 = pk2(bf2f(r1 & 0xffffu) + bs[2], bfhi(r1) + bs[3]);
      const float a[4] = {bf2f(a0 & 0xffffu), bfhi(a0), bf2f(a1 & 0xffffu), bfhi(a1)};
      float sg[4];
#pragma unroll
      for (int e = 0; e < 4; ++e) sg[e] = a[e] * __builtin_amdgcn_rcpf(1.f + __builtin_amdgcn_exp2f(-1.4426950408889634f * a[e]));
      gs[n].x = pk2(sg[0], sg[1]); gs[n].y = pk2(sg[2], sg[3]);
      asm volatile("" : "+v"(gs[n]));
    }
    STAMP(2); TQ(10);
    lds_wait(cnt + 9, 4u);
#pragma unroll
    for (int n = 0; n < 5; ++n) *(LAS v2u*)(sm + fslot(w, 0) + n * 512 + lane * 8) = gs[n];
    lds_signal(cnt + 10 + w, lane);
    STAMP(8); TQ(11);
    TS_OUT();
  }
}
}

extern "C" void kernel_launch(void* const* d_in, const int* in_sizes, int n_in,
                              void* d_out, int out_size, void* d_ws, size_t ws_size,
                              hipStream_t stream) {
  using s5k::u16;
  (void)hipFuncSetAttribute((const void*)s5k::s5_fused, hipFuncAttributeMaxDynamicSharedMemorySize, s5k::LDS_BYTES);
  s5k::s5_fused<<<s5k::NWG, s5k::NTHR, s5k::LDS_BYTES, stream>>>(
      (const u16*)d_in[0], (const u16*)d_in[1], (const u16*)d_in[2], (const u16*)d_in[3], (const u16*)d_in[4], (const u16*)d_in[5],
      (const u16*)d_in[6], (const u16*)d_in[7], (const u16*)d_in[8], (const u16*)d_in[10], (const u16*)d_in[11], (const u16*)d_in[12],
      (const u16*)d_in[13], (float*)d_out, (unsigned char*)d_ws);
}
```

```cpp
#include <hip/hip_runtime.h>
#include <hip/hip_bf16.h>
#include <stdint.h>
#include <type_traits>

namespace s5k {
typedef unsigned short u16;
typedef unsigned long long u64;
typedef unsigned v4u __attribute__((ext_vector_type(4)));
typedef unsigned v2u __attribute__((ext_vector_type(2)));
typedef float f32x4 __attribute__((ext_vector_type(4)));
typedef float f32x2 __attribute__((ext_vector_type(2)));
typedef float f32x16 __attribute__((ext_vector_type(16)));
typedef __bf16 bf16x8 __attribute__((ext_vector_type(8)));
typedef __bf16 bf16x2 __attribute__((ext_vector_type(2)));
typedef short s16x4 __attribute__((ext_vector_type(4)));
#define WAVE_ALL(c) (__builtin_amdgcn_ballot_w64(!(c)) == 0ull)
#define GAS __attribute__((address_space(1)))
#define LAS __attribute__((address_space(3)))
#define RLX_AGENT __ATOMIC_RELAXED, __HIP_MEMORY_SCOPE_AGENT
#define RLX_WG __ATOMIC_RELAXED, __HIP_MEMORY_SCOPE_WORKGROUP

constexpr int L = 2048, D = 640, P = 640, F = 1536;
constexpr int NWG = 256, NTHR = 512;
constexpr int EPOCH_OFF = 0;
constexpr int FLAG_E_OFF = 4096;
constexpr int FLAG_F_OFF = 65536;
constexpr int FLAG_H_OFF = 65536 + 8192;
constexpr int EG_OFF = 131072;
constexpr int PRS_OFF = 512 << 10;
constexpr int PCB_OFF = 576 << 10;
constexpr int PCF_OFF = 640 << 10;
constexpr int WT_OFF = 1 << 20;
constexpr int H1T_OFF = 8 << 20;
constexpr int HT_OFF = 16 << 20;
constexpr int WS_BYTES = 24 << 20;
constexpr int XS = 0, BWS = 16384, W1S = 26624, STAGE = 38912;
constexpr int XR = 3 * STAGE;
constexpr int XROW = XR, BROW = XR + 10240, W1SCR = BROW + 6400, W2SCR = W1SCR + 7680, CSCR = W2SCR + 7680, RAW = CSCR + 8192;
constexpr int MISC = 158976;
static_assert(RAW + 1024 <= MISC, "");
constexpr int ST2 = MISC;
constexpr int CSB = MISC + 1024;
constexpr int CSF = MISC + 2048;
constexpr int B1F = MISC + 2432;
constexpr int PRM = MISC + 2816;
constexpr int CNT = MISC + 4096;
constexpr int DB2 = MISC + 4160;
constexpr int HIN = CSB + 384;
constexpr int LDS_BYTES = MISC + 4864;
static_assert(HIN + 640 <= LDS_BYTES, "");
constexpr int SLOT0 = 40960;
constexpr int ESR = SLOT0 + 20 * 5120;
__device__ __forceinline__ int yslot(int w, int k) { return SLOT0 + (k ? 11 + w : (w == 3 ? 19 : 4 + w)) * 5120; }
__device__ __forceinline__ int fslot(int w, int k) { return SLOT0 + (k == 0 ? 15 + w : k == 1 ? w : 7 + w) * 5120; }
constexpr int SKR = ESR + 5120;
constexpr int JUNK = SKR + 10240;
static_assert(JUNK + 256 <= MISC, "");
static_assert(LDS_BYTES <= 163840, "");
constexpr unsigned SPIN_CAP = 1u << 16;
constexpr int AUX_SC1 = 16;

__device__ __forceinline__ float bf2f(unsigned b) { return __builtin_bit_cast(float, b << 16); }
__device__ __forceinline__ float bfhi(unsigned b) { return __builtin_bit_cast(float, b & 0xffff0000u); }
__device__ __forceinline__ unsigned pk2(float a, float b) { f32x2 f = {a, b}; return __builtin_bit_cast(unsigned, __builtin_convertvector(f, bf16x2)); }
__device__ __forceinline__ float rbf(float a) { return bf2f(pk2(a, a) & 0xffffu); }
__device__ __forceinline__ float dot2(unsigned a, unsigned b, float c) {
  return __builtin_amdgcn_fdot2_f32_bf16(__builtin_bit_cast(bf16x2, a), __builtin_bit_cast(bf16x2, b), c, false);
}
__device__ __forceinline__ float sum8(v4u a, float c) {
  const unsigned ones = 0x3f803f80u;
  c = dot2(a.x, ones, c); c = dot2(a.y, ones, c); c = dot2(a.z, ones, c); c = dot2(a.w, ones, c); return c;
}
__device__ __forceinline__ float sq8(v4u a, float c) {
  c = dot2(a.x, a.x, c); c = dot2(a.y, a.y, c); c = dot2(a.z, a.z, c); c = dot2(a.w, a.w, c); return c;
}
__device__ __forceinline__ float swp1(float v) {
  return __builtin_bit_cast(float, __builtin_amdgcn_update_dpp(0, __builtin_bit_cast(int, v), 0xB1, 0xF, 0xF, true));
}
template <int CTRL> __device__ __forceinline__ float dppf(float v) {
  return __builtin_bit_cast(float, __builtin_amdgcn_update_dpp(0, __builtin_bit_cast(int, v), CTRL, 0xF, 0xF, true));
}
__device__ __forceinline__ float add16(float v) {
  float c_ = v;
  asm("s_nop 1\n\tv_permlane16_swap_b32 %0, %1" : "+v"(v), "+v"(c_));
  return v + c_;
}
__device__ __forceinline__ float add32(float v) {
  float c_ = v;
  asm("s_nop 1\n\tv_permlane32_swap_b32 %0, %1" : "+v"(v), "+v"(c_));
  return v + c_;
}
__device__ __forceinline__ float sum32(float v) {
  v += dppf<0xB1>(v); v += dppf<0x4E>(v); v += dppf<0x141>(v); v += dppf<0x140>(v); return add16(v);
}
__device__ __forceinline__ f32x2 cmad(f32x2 lrr, f32x2 lsi, f32x2 h, f32x2 u) {
  return lrr * h + (lsi * __builtin_shufflevector(h, h, 1, 0) + u);
}
__device__ __forceinline__ f32x4 mfma16(v4u a, v4u b, f32x4 c) {
  return __builtin_amdgcn_mfma_f32_16x16x32_bf16(__builtin_bit_cast(bf16x8, a), __builtin_bit_cast(bf16x8, b), c, 0, 0, 0);
}
__device__ __forceinline__ f32x16 mfma32(v4u a, v4u b, f32x16 c) {
  return __builtin_amdgcn_mfma_f32_32x32x16_bf16(__builtin_bit_cast(bf16x8, a), __builtin_bit_cast(bf16x8, b), c, 0, 0, 0);
}
__device__ __forceinline__ v2u trread(LAS unsigned char* p) {
  return __builtin_bit_cast(v2u, __builtin_amdgcn_ds_read_tr16_b64_v4i16((LAS s16x4*)p));
}
__device__ __forceinline__ void dma1(const void* gb, unsigned vo, unsigned ld) {
  asm volatile("s_mov_b32 m0, %2\n\ts_nop 0\n\tglobal_load_lds_dwordx4 %1, %0" :: "s"(gb), "v"(vo), "s"(ld) : "memory");
}
__device__ __forceinline__ void dma1h(const void* gb, unsigned vo, unsigned ld) {
  asm volatile("s_mov_b32 m0, %2\n\ts_mov_b32 exec_hi, 0\n\tglobal_load_lds_dwordx4 %1, %0\n\ts_mov_b32 exec_hi, -1" :: "s"(gb), "v"(vo), "s"(ld) : "memory");
}
__device__ __forceinline__ void dma1s(const void* gb, unsigned vo, unsigned ld) {
  asm volatile("s_mov_b32 m0, %2\n\ts_nop 0\n\tglobal_load_lds_dwordx4 %1, %0 sc1" :: "s"(gb), "v"(vo), "s"(ld) : "memory");
}
template <int N> __device__ __forceinline__ void waitv() { asm volatile("s_waitcnt vmcnt(%0)" :: "i"(N) : "memory"); }
#define SCHED() __builtin_amdgcn_sched_barrier(0)
#define ABL_ST(x) x
#define WAITL0() asm volatile("s_waitcnt lgkmcnt(0)" ::: "memory")
#define BAR() __builtin_amdgcn_s_barrier()
#define LDU16(dst, ptr) asm volatile("global_load_ushort %0, %1, off" : "=v"(dst) : "v"(ptr) : "memory")
#define LDU32(dst, ptr) asm volatile("global_load_dword %0, %1, off" : "=v"(dst) : "v"(ptr) : "memory")
#define LDU64(dst, ptr) asm volatile("global_load_dwordx2 %0, %1, off" : "=v"(dst) : "v"(ptr) : "memory")
#define LDU128(dst, ptr) asm volatile("global_load_dwordx4 %0, %1, off" : "=v"(dst) : "v"(ptr) : "memory")

#define STAMP(k) do {} while (0)
#define TS(k) do {} while (0)
#define TQ(k) do {} while (0)
#define TG(k) do {} while (0)
#define TS_OUT() do {} while (0)

__device__ __forceinline__ u64 mix64(u64 z) {
  z += 0x9E3779B97F4A7C15ull; z = (z ^ (z >> 30)) * 0xBF58476D1CE4E5B9ull; z = (z ^ (z >> 27)) * 0x94D049BB133111EBull; return z ^ (z >> 31);
}
__device__ __forceinline__ void poll_flags(const GAS u64* f, u64 tag) {
#pragma nounroll
  for (unsigned spins = 0; spins < SPIN_CAP; ++spins) {
    const u64 v = __hip_atomic_load(f, RLX_AGENT);
    if (WAVE_ALL(v == tag)) break;
    __builtin_amdgcn_s_sleep(1);
  }
  __builtin_amdgcn_fence(__ATOMIC_ACQUIRE, "wavefront");
}
__device__ __forceinline__ void lds_signal(LAS unsigned* c, int lane) {
  WAITL0();
  if (lane == 0) __hip_atomic_fetch_add(c, 1u, RLX_WG);
}
__device__ __forceinline__ void lds_wait(LAS unsigned* c, unsigned want) {
#pragma nounroll
  for (unsigned spins = 0; spins < SPIN_CAP * 16; ++spins) {
    if (__hip_atomic_load(c, RLX_WG) >= want) break;
    __builtin_amdgcn_s_sleep(1);
  }
  asm volatile("" ::: "memory");
}
template <int NK> __device__ __forceinline__ int rotk(int k, int s) { const int a = k + s; return a >= NK ? a - NK : a; }
__device__ __forceinline__ v4u ldsc1(__amdgpu_buffer_rsrc_t rs, int voff, int soff) { return __builtin_amdgcn_raw_buffer_load_b128(rs, voff, soff, AUX_SC1); }

template <int NK, int DB, int DA, int NL, class LW, class HK, class HK2>
__device__ __forceinline__ void stream_gemm(__amdgpu_buffer_rsrc_t rs, int boff, const int (&aoff)[4], int s2, v4u (&bw)[DB][5], f32x4 (&acc)[5][4], LW lw, HK hook, HK2 hk2) {
  v4u aw[DA][4];
#pragma unroll
  for (int k = 0; k < DA; ++k) {
#pragma unroll
    for (int i = 0; i < 4; ++i) aw[k][i] = ldsc1(rs, aoff[i], rotk<NK>(k, s2) * 4096);
    SCHED();
  }
#pragma unroll
  for (int k = 0; k < NK; ++k) {
#pragma unroll
    for (int i = 0; i < 4; ++i) {
#pragma unroll
      for (int n = 0; n < 5; ++n) acc[n][i] = mfma16(bw[k % DB][n], aw[k % DA][i], acc[n][i]);
      SCHED();
      if (k + DA < NK) aw[k % DA][i] = ldsc1(rs, aoff[i], rotk<NK>(k + DA, s2) * 4096);
      SCHED();
    }
    if (k == NK - DA) hook();
    hk2(k);
    if (k + DB < NK) {
      if (k < NL) {
        LAS unsigned char* lp = lw(k);
#pragma unroll
        for (int n = 0; n < 5; ++n) bw[k % DB][n] = *(LAS v4u*)(lp + n * 1024);
      } else {
#pragma unroll
        for (int n = 0; n < 5; ++n) bw[k % DB][n] = ldsc1(rs, boff + n * 1024, rotk<NK>(k + DB, s2) * 5120);
      }
    }
    SCHED();
  }
}

#define ROT1 2
#define ROT2 0
#define DAF_ 3
#define DAY_ 3
constexpr int DBF = 5, DAF = DAF_, DBY = 5, DAY = DAY_, NLF = 3, NLY = 2;
constexpr int PBF = 2, PBY = 3;
__device__ __forceinline__ void dma1m(const void* gb, unsigned vo, unsigned ld, int sh) {
  asm volatile("s_mov_b32 m0, %2\n\ts_lshr_b64 exec, exec, %3\n\tglobal_load_lds_dwordx4 %1, %0\n\ts_mov_b64 exec, -1" :: "s"(gb), "v"(vo), "s"(ld), "s"(sh) : "memory", "scc");
}
constexpr int CODE_LINES = 344;
#define IC(n) std::integral_constant<int, n>{}

#if defined(__HIP_DEVICE_COMPILE__) && !defined(USE_PK)
#define NOPK_ATTR __attribute__((target("no-packed-fp32-ops")))
#else
#define NOPK_ATTR
#endif
__global__ __launch_bounds__(NTHR, 2) NOPK_ATTR void s5_fused(const u16* __restrict__ x, const u16* __restrict__ lre, const u16* __restrict__ lim,
    const u16* __restrict__ Bre, const u16* __restrict__ Bim, const u16* __restrict__ Cre, const u16* __restrict__ Cim,
    const u16* __restrict__ logdt, const u16* __restrict__ Dsk, const u16* __restrict__ w1, const u16* __restrict__ b1,
    const u16* __restrict__ w2, const u16* __restrict__ b2, float* __restrict__ out, unsigned char* __restrict__ ws) {
  extern __shared__ __attribute__((aligned(16))) unsigned char smem_[];
  LAS unsigned char* sm = (LAS unsigned char*)smem_;
  asm volatile("" :: "s"(x), "s"(lre), "s"(lim), "s"(Bre), "s"(Bim), "s"(Cre), "s"(Cim), "s"(logdt), "s"(Dsk), "s"(w1), "s"(b1), "s"(w2), "s"(b2), "s"(out), "s"(ws));
  u64 pc0; asm volatile("s_getpc_b64 %0" : "=s"(pc0));
  const int tid = threadIdx.x, lane = tid & 63;
  const int wv = __builtin_amdgcn_readfirstlane(tid >> 6), w = wv & 3;
  const int b = blockIdx.x, xcd = b & 7, idx = b >> 3;
  const int r = 4 * (xcd >> 1) + (idx & 3), jj = idx >> 2, j = 8 * (xcd & 1) + jj;
  const int half2 = jj & 1, ct = 4 * (xcd & 1) + (jj >> 1), rt64 = 2 * r + half2;
  const int s1 = ROT1 == 1 ? (j + 3 * r) % 10 : ROT1 == 2 ? 5 * ((j + r) & 1) : ROT1 == 3 ? 3 * ((j + r) % 3) : 0;
  STAMP(0); TS(0); TG(0);
  const unsigned smb = (unsigned)(size_t)sm;
  const __amdgpu_buffer_rsrc_t rs = __builtin_amdgcn_make_buffer_rsrc((void*)ws, 0, WS_BYTES, 0x00020000);
  LAS unsigned* cnt = (LAS unsigned*)(sm + CNT);
  const int n16 = lane & 15, g = lane >> 4;
  const int s2f = ROT2 ? 3 * (ct & 3) : 0, s2y = ROT2 ? (5 * (ct & 3)) >> 1 : 0;
  const int ctu = b & 7, fu = b >> 3;
  auto read_tag = [&]() __attribute__((always_inline)) {
    const v2u e = *(LAS v2u*)(sm + RAW + 47 * 16);
    return mix64(((u64)__builtin_amdgcn_readfirstlane(e.y) << 32) | (unsigned)__builtin_amdgcn_readfirstlane(e.x));
  };

  if (wv >= 4) {
    {
      const int line = (idx * 4 + w) * 3 + (lane < 3 ? lane : 2);
      if (line < CODE_LINES)
        asm volatile("s_mov_b32 m0, %2\n\ts_nop 0\n\tglobal_load_lds_dword %1, %0" :: "s"(pc0), "v"(line * 128), "s"(smb + JUNK) : "memory");
    }
  }
  if (__builtin_expect(wv < 4, 1)) {
    unsigned so[10];
    { const int xrow = 32 * w + (lane >> 3);
      const unsigned xb = (unsigned)(128 * r + xrow) * 1280u + (((lane & 7) ^ ((xrow >> 1) & 1)) << 4);
#pragma unroll
      for (int q = 0; q < 4; ++q) so[q] = (xb + q * 10240u) ^ (q << 5); }
    const unsigned char* bmat = (const unsigned char*)((w >> 1) ? Bim : Bre);
    const unsigned l16 = lane * 16;
    auto job = [&](int d) __attribute__((always_inline)) {
      if (d < 100) {
        const int st = d / 10, p = d % 10, kt = rotk<10>(st, s1);
        const unsigned sb = smb + (st % 3) * STAGE;
        if (p < 4) dma1((const unsigned char*)x + kt * 128, so[p], sb + XS + (32 * w + 8 * p) * 128);
        else if (p < 6) dma1(bmat + kt * 128, so[p], sb + BWS + 2560 * w + (p - 4) * 1024);
        else if (p == 6) dma1h(bmat + kt * 128, so[p], sb + BWS + 2560 * w + 2048);
        else dma1((const unsigned char*)w1 + (size_t)kt * (64 * 3072), so[p], sb + W1S + 3072 * w + (p - 7) * 1024);
      } else if (d < 102) {
        const int pc = 2 * w + (d - 100);
        dma1s(ws + (pc == 0 ? PRS_OFF + r * 1024 : pc == 1 ? PCB_OFF + j * 1024 : PCF_OFF + j * 6144 + (pc - 2) * 1024), l16,
              smb + (pc == 0 ? ST2 : pc == 1 ? CSB : ESR + (pc - 2) * 1024));
      } else if (d < 127) {
        const int sl = (d - 102) / 5, n = (d - 102) % 5;
        if (sl == 2 || sl == 4) dma1s(ws + WT_OFF + (ct * 88 + 10 * w + rotk<10>(DBY + (sl >> 2), s2y)) * 5120 + n * 1024, l16, smb + yslot(w, sl >> 2) + n * 1024);
        else { const int k = sl == 3 ? 2 : sl; dma1s(ws + WT_OFF + (ct * 88 + 40 + 12 * w + rotk<12>(DBF + k, s2f)) * 5120 + n * 1024, l16, smb + fslot(w, k) + n * 1024); }
      }
    };
#pragma unroll
    for (int d = 0; d < 4; ++d) job(d);
#pragma unroll
    for (int q3 = 0; q3 < 3; ++q3) {
      int ch = 64 * q3 + lane; if (ch > 159) ch = 159;
      const int cl = 20 * (w & 1) + (ch >> 3), cs_ = ch & 7;
      so[4 + q3] = (unsigned)(40 * j + cl) * 1280u + ((cs_ ^ ((cl >> 1) & 7) ^ ((w >> 1) << 2)) << 4);
      const int c2 = 192 * w + 64 * q3 + lane;
      so[7 + q3] = (unsigned)(c2 / 12) * 3072u + j * 192 + (c2 % 12) * 16;
    }
#pragma unroll
    for (int d = 4; d < 10; ++d) job(d);
    if (tid < 16) cnt[tid] = 0u;
    const unsigned o1 = (unsigned)(40 * r + lane / 12) * 3072u + j * 192 + (lane % 12) * 16;
    const unsigned o2 = (unsigned)(48 * fu + 12 * w + lane / 10) * 1280u + ctu * 160 + (lane % 10) * 16;
    const int u = b * 4 + w, cu = u < 800 ? u : u - 800, cd8 = cu / 10, ccb = cu % 10;
    const unsigned o3 = (unsigned)((8 * cd8 + (lane >> 3)) * P + 64 * ccb + 8 * (lane & 7)) * 2;
    auto jobG2 = [&](int q) __attribute__((always_inline)) {
      const int p = 7 * w + q;
      if (p < 25) {
        const unsigned char* gb = p < 10 ? (const unsigned char*)x + (size_t)(128 * r + 8 * j) * 1280 + p * 1024
                                : p < 17 ? (const unsigned char*)(r < 8 ? Bre : Bim) + (size_t)(40 * j + 5 * (r & 7)) * 1280 + (p - 10) * 1024
                                         : (const unsigned char*)w1 + (p - 17) * (5 * 3072);
        dma1m(gb, p < 17 ? l16 : o1, smb + (p < 17 ? XROW + p * 1024 : W1SCR + (p - 17) * 960), p < 16 ? 0 : p == 16 ? 48 : 4);
      } else if (p == 25) {
        const u16* vb = lane < 5 ? lre + 40 * j + 8 * lane : lane < 10 ? lim + 40 * j + 8 * (lane - 5) : lane < 15 ? logdt + 40 * j + 8 * (lane - 10)
                      : lane < 27 ? b1 + 96 * j + 8 * (lane - 15) : lane < 37 ? Dsk + 80 * ct + 8 * (lane - 27) : lane < 47 ? b2 + 80 * ct + 8 * (lane - 37) : (const u16*)(ws + EPOCH_OFF);
        asm volatile("s_mov_b32 m0, %1\n\ts_nop 0\n\tglobal_load_lds_dwordx4 %0, off sc1" :: "v"(vb), "s"(smb + RAW) : "memory");
      }
    };
    auto jobG1 = [&](int q) __attribute__((always_inline)) {
      if (q < 2) dma1m((const unsigned char*)w2 + q * (6 * 1280), o2, smb + W2SCR + (2 * w + q) * 960, 4);
      else dma1(q == 2 ? Cre : Cim, o3, smb + CSCR + w * 2048 + (q - 2) * 1024);
    };
    const int rho = n16;
    const int Rrow = 32 * w + 8 * (rho >> 2) + (rho & 3);
    const int fx = ((rho >> 2) << 1) | ((rho >> 1) & 1);
    const int xa0 = XS + Rrow * 128 + ((g ^ fx) << 4);
    const int wb0 = BWS + rho * 128 + ((g ^ ((rho >> 1) & 7)) << 4);
    const int wb4 = BWS + (40 * (rho & 1) + 32 + (rho >> 1)) * 128 + ((g ^ (rho >> 2) ^ ((rho & 1) << 2)) << 4);
    f32x4 acc[2][5];
#pragma unroll
    for (int i = 0; i < 2; ++i)
#pragma unroll
      for (int t = 0; t < 5; ++t) acc[i][t] = f32x4{0.f, 0.f, 0.f, 0.f};
    v4u fa[2][2][2], fb[2][2][5];
    auto rdB = [&](int set, int st, int q) __attribute__((always_inline)) {
      LAS unsigned char* sb = sm + (st % 3) * STAGE;
      const int kk = q / 7, e = q % 7;
      if (e < 2) fa[set][kk][e] = *(LAS v4u*)(sb + (xa0 ^ (kk * 64)) + e * 512);
      else if (e < 6) { const int t = e - 2; fb[set][kk][t] = *(LAS v4u*)(sb + (wb0 ^ ((kk ^ (t & 1)) * 64)) + (t & 1) * 5120 + (t >> 1) * 2048); }
      else fb[set][kk][4] = *(LAS v4u*)(sb + (wb4 ^ (kk * 64)));
    };
    auto mmB = [&](int set, int m) __attribute__((always_inline)) {
      const int kk = m / 10, t = (m % 10) >> 1, i = m & 1;
      acc[i][t] = mfma16(fa[set][kk][i], fb[set][kk][t], acc[i][t]);
    };
    jobG2(0); jobG2(1);
    waitv<2>();
    BAR();
    STAMP(9);
#pragma unroll
    for (int q = 2; q < 7; ++q) jobG2(q);
#pragma unroll
    for (int q = 0; q < 4; ++q) jobG1(q);
#pragma unroll
    for (int q = 0; q < 14; ++q) rdB(0, 0, q);
    SCHED();
#pragma unroll
    for (int m = 0; m < 20; ++m) { mmB(0, m); if (m & 1) job(10 + (m >> 1)); SCHED(); }
    waitv<10>(); BAR();
#pragma unroll
    for (int d = 20; d < 30; ++d) job(d);
    {
      LAS unsigned char* sc = sm + CSCR + w * 2048 + (lane & 7) * 128 + (lane >> 3) * 8;
      const v2u r0 = *(LAS v2u*)sc, i0 = *(LAS v2u*)(sc + 1024), r1 = *(LAS v2u*)(sc + 64), i1 = *(LAS v2u*)(sc + 1088);
      v4u o0, o1_;
      o0.x = __builtin_amdgcn_perm(i0.x, r0.x, 0x05040100u); o0.y = __builtin_amdgcn_perm(i0.x, r0.x, 0x07060302u);
      o0.z = __builtin_amdgcn_perm(i0.y, r0.y, 0x05040100u); o0.w = __builtin_amdgcn_perm(i0.y, r0.y, 0x07060302u);
      o1_.x = __builtin_amdgcn_perm(i1.x, r1.x, 0x05040100u); o1_.y = __builtin_amdgcn_perm(i1.x, r1.x, 0x07060302u);
      o1_.z = __builtin_amdgcn_perm(i1.y, r1.y, 0x05040100u); o1_.w = __builtin_amdgcn_perm(i1.y, r1.y, 0x07060302u);
      const int ctc = cd8 / 10, ntc = (cd8 % 10) >> 1;
      const int fo = WT_OFF + ((ctc * 88 + 4 * ccb + (lane >> 5)) * 5 + ntc) * 1024 + ((lane >> 3) & 3) * 256 + (8 * (cd8 & 1) + (lane & 7)) * 16;
      __builtin_amdgcn_raw_buffer_store_b128(o0, rs, fo, 0, AUX_SC1);
      __builtin_amdgcn_raw_buffer_store_b128(o1_, rs, fo + 2 * 5120, 0, AUX_SC1);
      SCHED();
    }
#pragma unroll
    for (int d = 30; d < 40; ++d) job(d);
    TS(1);
    waitv<22>(); TS(2); BAR(); TS(3);
#pragma unroll
    for (int q = 0; q < 14; ++q) rdB(1, 1, q);
    WAITL0(); waitv<12>(); BAR();
#pragma unroll
    for (int d = 40; d < 44; ++d) job(d);
    const u64 tag = read_tag();
    v4u efv[2];
    const int boff = WT_OFF + (ct * 88 + 10 * w) * 5120 + lane * 16;
    const int s2 = s2y;
    v4u bw[DBY][5];
    const int q = g, c = n16;
    LAS float* pm = (LAS float*)(sm + PRM);
    f32x4 st0, st1, st2_, st3; float Sc[5]; f32x2 lr2, li2; float lr4, ls4;
    auto stepB = [&](auto I_) __attribute__((always_inline)) {
      constexpr int i = decltype(I_)::value;
      auto jobA = [&](int a) __attribute__((always_inline)) {
        if (i <= 6) { if (a < 6) job(10 * (i + 3) + 4 + a); }
        else if (i == 7) job(106 + a);
        else if (i == 8) job(119 + a);
        else if (a < 5) bw[1][a] = ldsc1(rs, boff + a * 1024, rotk<10>(1, s2) * 5120);
      };
      auto jobB = [&](int a) __attribute__((always_inline)) {
        if (i <= 5) { if (a < 4) job(10 * (i + 4) + a); }
        else if (i == 6) job(100 + a);
        else if (i == 7) job(113 + a);
        else if (i == 8) { if (a == 0) job(126); else bw[0][a - 1] = ldsc1(rs, boff + (a - 1) * 1024, s2 * 5120); }
        else if (a < 5) bw[2][a] = ldsc1(rs, boff + a * 1024, rotk<10>(2, s2) * 5120);
      };
      if (i == 9) {
        LAS f32x4* sp = (LAS f32x4*)(sm + ST2 + (32 * w + 8 * q) * 8);
        st0 = sp[0]; st1 = sp[1]; st2_ = sp[2]; st3 = sp[3];
#pragma unroll
        for (int t = 0; t < 5; ++t) Sc[t] = ((LAS float*)(sm + CSB))[16 * t + c];
        lr2 = f32x2{pm[c], pm[16 + c]}; li2 = f32x2{pm[40 + c], pm[56 + c]};
        lr4 = pm[32 + (c >> 1)]; ls4 = pm[72 + (c >> 1)];
        SCHED();
      }
#pragma unroll
      for (int m = 0; m < 14; ++m) { mmB(i & 1, m); if (i < 9) rdB((i + 1) & 1, i + 1, m); if (m & 1) jobA(m >> 1); SCHED(); }
      if (i < 9) {
        WAITL0();
        TS(1 + 3 * i);
        if (i < 6) waitv<10>();
        else if (i == 6) {
          asm volatile("s_waitcnt vmcnt(10)" : "+v"(efv[0]), "+v"(efv[1]) :: "memory");
          if (w == 1) {
            const unsigned tl = (unsigned)tag, th = (unsigned)(tag >> 32);
            const unsigned bad = (efv[0].x ^ tl) | (efv[0].y ^ th) | (efv[0].z ^ tl) | (efv[0].w ^ th) | (efv[1].x ^ tl) | (efv[1].y ^ th) | (efv[1].z ^ tl) | (efv[1].w ^ th);
            if (!WAVE_ALL(bad == 0u)) {
#pragma nounroll
              for (unsigned spins = 0; spins < SPIN_CAP; ++spins) {
                const v4u e0 = ldsc1(rs, FLAG_E_OFF + xcd * 2048 + lane * 32, 0), e1 = ldsc1(rs, FLAG_E_OFF + xcd * 2048 + lane * 32 + 16, 0);
                if (WAVE_ALL(((e0.x ^ tl) | (e0.y ^ th) | (e0.z ^ tl) | (e0.w ^ th) | (e1.x ^ tl) | (e1.y ^ th) | (e1.z ^ tl) | (e1.w ^ th)) == 0u)) break;
                asm volatile("s_sleep 2" ::: "memory");
              }
            }
            if (b == 0 && lane == 0) __hip_atomic_store((GAS u64*)(ws + EPOCH_OFF), tag, RLX_AGENT);
          }
        }
        else if (i == 7) waitv<13>();
        else waitv<24>();
        TS(2 + 3 * i);
        BAR();
        TS(3 + 3 * i);
      }
      if (i == 2 && tid < 8) __hip_atomic_store((GAS u64*)(ws + FLAG_E_OFF) + tid * 256 + b, tag, RLX_AGENT);
      if (i == 5) {
        asm volatile("s_cmp_eq_u32 %4, 1\n\ts_cselect_b64 exec, -1, 0\n\tglobal_load_dwordx4 %0, %2, %3 sc1\n\tglobal_load_dwordx4 %1, %2, %3 offset:16 sc1\n\ts_mov_b64 exec, -1"
                     : "=&v"(efv[0]), "=&v"(efv[1]) : "v"(l16 * 2), "s"(ws + FLAG_E_OFF + xcd * 2048), "s"(w) : "memory", "scc");
      }
      if (i == 8 && w == 3) {
        const int l48 = lane < 48 ? lane : 47;
        f32x2 a = *(LAS f32x2*)(sm + ESR + l48 * 8);
#pragma unroll
        for (int k = 1; k < 16; ++k) a += *(LAS f32x2*)(sm + ESR + k * 384 + l48 * 8);
        *(LAS f32x2*)(sm + CSF + l48 * 8) = a;
        lds_signal(cnt + 8, lane);
      }
      SCHED();
#pragma unroll
      for (int m = 14; m < 20; ++m) { mmB(i & 1, m); jobB(m - 14); SCHED(); }
    };
    stepB(IC(1)); stepB(IC(2)); stepB(IC(3)); stepB(IC(4)); stepB(IC(5)); stepB(IC(6)); stepB(IC(7)); stepB(IC(8)); stepB(IC(9));
    STAMP(1); TQ(0);
    __builtin_amdgcn_s_setprio(2);
    f32x2 ur[8], ui[8], er, ei;
    float u4[8], e4;
    ls4 = (c & 1) ? ls4 : -ls4;
    {
      const float rsa[4] = {st0[0], st0[2], st1[0], st1[2]}, rsb[4] = {st2_[0], st2_[2], st3[0], st3[2]};
      f32x2 mba[4] = {{st0[1], st0[1]}, {st0[3], st0[3]}, {st1[1], st1[1]}, {st1[3], st1[3]}}, mbb[4] = {{st2_[1], st2_[1]}, {st2_[3], st2_[3]}, {st3[1], st3[1]}, {st3[3], st3[3]}};
#pragma unroll
      for (int s = 0; s < 4; ++s) { asm volatile("" : "+v"(mba[s])); asm volatile("" : "+v"(mbb[s])); }
      const f32x2 Sr = {Sc[0], Sc[2]}, Si = {Sc[1], Sc[3]};
#pragma unroll
      for (int s = 0; s < 4; ++s) {
        ur[s] = (f32x2{acc[0][0][s], acc[0][2][s]} - mba[s] * Sr) * rsa[s]; ui[s] = (f32x2{acc[0][1][s], acc[0][3][s]} - mba[s] * Si) * rsa[s];
        ur[4 + s] = (f32x2{acc[1][0][s], acc[1][2][s]} - mbb[s] * Sr) * rsb[s]; ui[4 + s] = (f32x2{acc[1][1][s], acc[1][3][s]} - mbb[s] * Si) * rsb[s];
        u4[s] = (acc[0][4][s] - mba[s][0] * Sc[4]) * rsa[s]; u4[4 + s] = (acc[1][4][s] - mbb[s][0] * Sc[4]) * rsb[s];
      }
      er = ur[0]; ei = ui[0]; e4 = u4[0];
#pragma unroll
      for (int s = 1; s < 8; ++s) {
        const f32x2 nr = lr2 * er + (ur[s] - li2 * ei), ni = lr2 * ei + (li2 * er + ui[s]);
        er = nr; ei = ni;
        e4 = lr4 * e4 + (ls4 * swp1(e4) + u4[s]);
      }
    }
    lds_wait(cnt + 8, 1u);
    LAS float* es = (LAS float*)(sm + ESR);
    es[(4 * w + q) * 80 + 2 * c] = er[0]; es[(4 * w + q) * 80 + 2 * c + 1] = ei[0];
    es[(4 * w + q) * 80 + 32 + 2 * c] = er[1]; es[(4 * w + q) * 80 + 33 + 2 * c] = ei[1];
    es[(4 * w + q) * 80 + 64 + c] = e4;
    lds_signal(cnt + 0, lane);
    TQ(1);
    const int l40 = lane < 40 ? lane : 39;
    const unsigned tlo = (unsigned)tag >> 24 << 24, thi = (unsigned)(tag >> 32);
    if (w == 0) {
      const float l8r = pm[80 + l40], l8i = pm[120 + l40];
      lds_wait(cnt + 0, 4u);
      TQ(2);
      f32x2 ev[16];
#pragma unroll
      for (int s = 0; s < 16; ++s) ev[s] = *(LAS f32x2*)(es + s * 80 + 2 * l40);
      float Er = 0.f, Ei = 0.f;
#pragma unroll
      for (int s = 0; s < 16; ++s) {
        const float pr_ = Er, pi_ = Ei;
        Er = __builtin_fmaf(l8r, pr_, __builtin_fmaf(-l8i, pi_, ev[s][0])); Ei = __builtin_fmaf(l8r, pi_, __builtin_fmaf(l8i, pr_, ev[s][1]));
        asm volatile("" : "+v"(Er)); asm volatile("" : "+v"(Ei));
        ev[s] = f32x2{pr_, pi_};
      }
      if (r < 15 && lane < 40) {
        v4u o; o.x = tlo | ((__builtin_bit_cast(unsigned, Er) + 0x80u) >> 8); o.y = thi; o.z = tlo | ((__builtin_bit_cast(unsigned, Ei) + 0x80u) >> 8); o.w = thi;
        __builtin_amdgcn_raw_buffer_store_b128(o, rs, EG_OFF + ((j * 16 + r) * 40 + lane) * 16, 0, AUX_SC1);
      }
      STAMP(2); TQ(3);
#pragma unroll
      for (int s = 0; s < 16; ++s) *(LAS f32x2*)(es + s * 80 + 2 * l40) = ev[s];
    }
    waitv<15>();
    if (lane == 0) __hip_atomic_fetch_add(cnt + 4 + w, 1u, RLX_WG);
#pragma unroll
    for (int k = PBY; k < DBY; ++k)
#pragma unroll
      for (int n = 0; n < 5; ++n) bw[k][n] = ldsc1(rs, boff + n * 1024, rotk<10>(k, s2) * 5120);
    SCHED();
    f32x2 gr = {1.f, 1.f}, gi = {0.f, 0.f}, g4r = {1.f, 1.f}, g4i = {0.f, 0.f};
    { f32x2 pr = {pm[80 + c], pm[96 + c]}, pi = {pm[120 + c], pm[136 + c]};
      f32x2 qr = {pm[112 + (c >> 1)], 0.f}, qi = {pm[152 + (c >> 1)], 0.f};
      const int sg = 4 * w + q;
#pragma unroll
      for (int bt = 0; bt < 4; ++bt) {
        const f32x2 tr_ = gr * pr - gi * pi, ti_ = gr * pi + gi * pr, ur_ = g4r * qr - g4i * qi, ui_ = g4r * qi + g4i * qr;
        if ((sg >> bt) & 1) { gr = tr_; gi = ti_; g4r = ur_; g4i = ui_; }
        if (bt < 3) { const f32x2 n1 = pr * pr - pi * pi, n2 = qr * qr - qi * qi; pi = 2.f * pr * pi; pr = n1; qi = 2.f * qr * qi; qr = n2; }
      } }
    const float g4s = (c & 1) ? g4i[0] : -g4i[0];
    const f32x2 cr2 = f32x2{2.f * pm[240 + c], 2.f * pm[256 + c]}, ci2 = f32x2{2.f * pm[280 + c], 2.f * pm[296 + c]};
    const float cr4 = pm[272 + (c >> 1)], ca4 = (c & 1) ? -2.f * cr4 : 2.f * cr4, cb4 = -2.f * pm[312 + (c >> 1)];
    int hoffs[5];
#pragma unroll
    for (int m = 0; m < 5; ++m) {
      const int unit = 4 * m + g, kap = 10 * j + (unit >> 1);
      hoffs[m] = HT_OFF + (((2 * r + (w >> 1)) * 40 + (kap >> 2)) * 4 + 2 * (w & 1) + (unit & 1)) * 1024 + (kap & 3) * 256 + n16 * 16;
      asm volatile("" : "+v"(hoffs[m]));
    }
    TQ(4);
    if (w < 2) {
      float Hr_ = 0.f, Hi_ = 0.f;
      if (r > 8 * w) {
        const float lKr = pm[160 + l40], lKi = pm[200 + l40];
        float p4r, p4i, p8r, p8i;
        { const float a2r = lKr * lKr - lKi * lKi, a2i = 2.f * lKr * lKi; p4r = a2r * a2r - a2i * a2i; p4i = 2.f * a2r * a2i; p8r = p4r * p4r - p4i * p4i; p8i = 2.f * p4r * p4i; }
        const int eo = EG_OFF + (j * 16 * 40 + l40) * 16, top = r - 1 - 8 * w;
        v4u gv[8];
#pragma nounroll
        for (unsigned spins = 0; spins < SPIN_CAP; ++spins) {
#pragma unroll
          for (int m = 0; m < 8; ++m) gv[m] = ldsc1(rs, eo, (m <= top ? top - m : 0) * 640);
          unsigned bad = 0u, bad2 = 0u;
#pragma unroll
          for (int m = 0; m < 8; ++m) { bad |= (gv[m].x ^ tlo) | (gv[m].z ^ tlo); bad2 |= (gv[m].y ^ thi) | (gv[m].w ^ thi); }
          if (WAVE_ALL(((bad >> 24) | bad2) == 0u)) break;
          asm volatile("s_sleep 1" ::: "memory");
        }
        float hr_[2] = {0.f, 0.f}, hi_[2] = {0.f, 0.f};
#pragma unroll
        for (int m = 3; m >= 0; --m)
#pragma unroll
          for (int k = 0; k < 2; ++k) {
            const bool ok = 4 * k + m <= top;
            const float er_ = ok ? __builtin_bit_cast(float, gv[4 * k + m].x << 8) : 0.f, ei_ = ok ? __builtin_bit_cast(float, gv[4 * k + m].z << 8) : 0.f;
            const float nr_ = __builtin_fmaf(lKr, hr_[k], __builtin_fmaf(-lKi, hi_[k], er_)), ni_ = __builtin_fmaf(lKr, hi_[k], __builtin_fmaf(lKi, hr_[k], ei_));
            hr_[k] = nr_; hi_[k] = ni_;
            asm volatile("" : "+v"(hr_[k])); asm volatile("" : "+v"(hi_[k]));
          }
        Hr_ = p4r * hr_[1] - p4i * hi_[1] + hr_[0]; Hi_ = p4r * hi_[1] + p4i * hr_[1] + hi_[0];
        asm volatile("" : "+v"(Hr_)); asm volatile("" : "+v"(Hi_));
        if (w == 1) { const float t_ = p8r * Hr_ - p8i * Hi_; Hi_ = p8r * Hi_ + p8i * Hr_; Hr_ = t_; asm volatile("" : "+v"(Hr_)); asm volatile("" : "+v"(Hi_)); }
      }
      STAMP(3);
      ((LAS float*)(sm + HIN))[80 * w + 2 * l40] = Hr_; ((LAS float*)(sm + HIN))[80 * w + 2 * l40 + 1] = Hi_;
      lds_signal(cnt + 1, lane);
    }
    TQ(5);
    {
      lds_wait(cnt + 1, 2u);
      TQ(6);
      LAS unsigned char* scr = sm + w * 10240;
      LAS float* hin = (LAS float*)(sm + HIN);
      const f32x2 Hr = {hin[2 * c] + hin[80 + 2 * c], hin[32 + 2 * c] + hin[112 + 2 * c]}, Hi = {hin[2 * c + 1] + hin[81 + 2 * c], hin[33 + 2 * c] + hin[113 + 2 * c]};
      f32x2 hr = {es[(4 * w + q) * 80 + 2 * c], es[(4 * w + q) * 80 + 32 + 2 * c]}, hi = {es[(4 * w + q) * 80 + 2 * c + 1], es[(4 * w + q) * 80 + 33 + 2 * c]};
      hr += gr * Hr - gi * Hi; hi += gr * Hi + gi * Hr;
      float h4 = es[(4 * w + q) * 80 + 64 + c] + (g4r[0] * (hin[64 + c] + hin[144 + c]) + g4s * (hin[64 + (c ^ 1)] + hin[144 + (c ^ 1)])), hp4 = swp1(h4);
      f32x2 o2[2][8]; float o4[8];
#pragma unroll
      for (int s = 0; s < 8; ++s) {
        const f32x2 nr = lr2 * hr + (ur[s] - li2 * hi), ni = lr2 * hi + (li2 * hr + ui[s]);
        hr = nr; hi = ni;
        o2[0][s] = cr2 * hr - ci2 * hi;
        o2[1][s] = -(cr2 * hi + ci2 * hr);
        h4 = lr4 * h4 + (ls4 * hp4 + u4[s]); hp4 = swp1(h4);
        o4[s] = ca4 * h4 + cb4 * hp4;
      }
#pragma unroll
      for (int p = 0; p < 2; ++p)
#pragma unroll
        for (int pt = 0; pt < 2; ++pt) {
          v4u ov; ov.x = pk2(o2[pt][0][p], o2[pt][1][p]); ov.y = pk2(o2[pt][2][p], o2[pt][3][p]); ov.z = pk2(o2[pt][4][p], o2[pt][5][p]); ov.w = pk2(o2[pt][6][p], o2[pt][7][p]);
          *(LAS v4u*)(scr + (32 * p + 2 * c + pt) * 80 + q * 16) = ov;
        }
      { v4u ov; ov.x = pk2(o4[0], o4[1]); ov.y = pk2(o4[2], o4[3]); ov.z = pk2(o4[4], o4[5]); ov.w = pk2(o4[6], o4[7]);
        *(LAS v4u*)(scr + (64 + c) * 80 + q * 16) = ov; }
      WAITL0();
      TQ(7);
      LAS unsigned char* ra = scr + (8 * (g >> 1) + ((lane & 15) >> 2)) * 80 + (16 * (g & 1) + 4 * (lane & 3)) * 2;
      v2u lo[5], hi_[5];
#pragma unroll
      for (int m = 0; m < 5; ++m) { lo[m] = trread(ra + m * 1280); hi_[m] = trread(ra + m * 1280 + 320); }
#pragma unroll
      for (int m = 0; m < 5; ++m) {
        v4u ov; ov.x = lo[m].x; ov.y = lo[m].y; ov.z = hi_[m].x; ov.w = hi_[m].y;
        __builtin_amdgcn_raw_buffer_store_b128(ov, rs, hoffs[m], 0, AUX_SC1);
      }
    }
    STAMP(4); TQ(8);
    SCHED();
    waitv<0>();
    if (lane == 0) __hip_atomic_store((GAS u64*)(ws + FLAG_H_OFF) + ((r * 2 + (w >> 1)) * 16 + j) * 2 + (w & 1), tag, RLX_AGENT);
    STAMP(5); TQ(9);
    __builtin_amdgcn_s_setprio(0);
    TQ(10);
    poll_flags((const GAS u64*)(ws + FLAG_H_OFF) + ((r * 2 + half2) * 16 + 4 * w) * 2 + (lane & 7), tag);
    STAMP(6); TQ(11); TG(1);
    f32x4 ac2[5][4];
#pragma unroll
    for (int n = 0; n < 5; ++n)
#pragma unroll
      for (int i = 0; i < 4; ++i) ac2[n][i] = f32x4{0.f, 0.f, 0.f, 0.f};
    int aoff[4];
#pragma unroll
    for (int i = 0; i < 4; ++i) aoff[i] = HT_OFF + ((rt64 * 40 + 10 * w) * 4 + ((i + w) & 3)) * 1024 + lane * 16;
    const size_t orow = (size_t)(128 * r + 64 * half2 + 16 * w + n16) * D + 80 * ct + 4 * g;
    v2u xv[5];
    stream_gemm<10, DBY, DAY, NLY>(rs, boff, aoff, s2, bw, ac2,
        [&](int k) __attribute__((always_inline)) { return sm + yslot(w, k) + lane * 16; },
        [&]() __attribute__((always_inline)) {
#pragma unroll
          for (int n = 0; n < 5; ++n) xv[n] = *(const v2u*)(x + orow + 16 * n);
        }, [&](int k) __attribute__((always_inline)) { TG(2 + k); });
    STAMP(7); TQ(12);
    auto ypart = [&](int ww, int i) __attribute__((always_inline)) { return sm + (i < 2 ? ww * 10240 + i * 5120 : yslot(ww, 0)) + lane * 16; };
#pragma unroll
    for (int i = 1; i < 4; ++i)
#pragma unroll
      for (int n = 0; n < 5; ++n) *(LAS f32x4*)(ypart(w, i - 1) + n * 1024) = ac2[n][i];
    lds_signal(cnt + 3, lane);
    TQ(13);
    lds_wait(cnt + 10 + w, 1u);
    v4u gs[5];
#pragma unroll
    for (int n = 0; n < 5; ++n) {
      const v2u gq = *(LAS v2u*)(sm + fslot(w, 0) + n * 512 + lane * 8), sq = *(LAS v2u*)(sm + SKR + w * 2560 + n * 512 + lane * 8);
      gs[n] = v4u{gq.x, gq.y, sq.x, sq.y};
    }
    TQ(14);
    lds_wait(cnt + 3, 4u);
    TQ(15);
#pragma unroll
    for (int n = 0; n < 5; ++n) {
      f32x4 y = ac2[n][0] + f32x4{bf2f(gs[n].z & 0xffffu), bfhi(gs[n].z), bf2f(gs[n].w & 0xffffu), bfhi(gs[n].w)};
#pragma unroll
      for (int o = 1; o < 4; ++o) y += *(LAS f32x4*)(ypart((w + o) & 3, 3 - o) + n * 1024);
      const f32x4 gg = {bf2f(gs[n].x & 0xffffu), bfhi(gs[n].x), bf2f(gs[n].y & 0xffffu), bfhi(gs[n].y)};
      const f32x4 xx = {bf2f(xv[n].x & 0xffffu), bfhi(xv[n].x), bf2f(xv[n].y & 0xffffu), bfhi(xv[n].y)};
      __builtin_nontemporal_store(xx + y * gg, (f32x4*)(out + orow + 16 * n));
    }
    STAMP(8); TQ(16);
    TS_OUT();
  } else {
    if (w == 0) {
      int o;
      if (lane < 8) o = FLAG_E_OFF + lane * 2048 + b * 8;
      else if (lane < 12) o = FLAG_F_OFF + (r * 2 + ((lane >> 1) & 1)) * 256 + (lane & 1) * 128;
      else if (lane < 16) o = FLAG_H_OFF + (r * 2 + ((lane >> 1) & 1)) * 256 + (lane & 1) * 128;
      else if (lane < 21) o = EG_OFF + (j * 16 + r) * 640 + (lane - 16) * 128;
      else if (lane < 24) o = PCF_OFF + (j * 16 + r) * 384 + (lane - 21) * 128;
      else if (lane < 27) o = PCB_OFF + j * 1024 + (lane - 24) * 128;
      else o = PRS_OFF + r * 1024 + j * 64;
      asm volatile("s_mov_b32 m0, %2\n\ts_nop 0\n\tglobal_load_lds_dword %1, %0 sc1" :: "s"(ws), "v"(o), "s"(smb + JUNK) : "memory");
    }
    const int t32 = lane & 31, hh = lane >> 5;
    const int fx = (((t32 >> 3) & 3) << 1) | ((t32 >> 1) & 1);
    const int xa0 = XS + (32 * w + t32) * 128 + ((hh ^ fx) << 4);
    const int wa0 = W1S + (8 * hh + ((lane & 15) >> 2)) * 192 + (16 * ((lane >> 4) & 1) + 4 * (lane & 3)) * 2;
    f32x16 acc[3];
#pragma unroll
    for (int n = 0; n < 3; ++n)
#pragma unroll
      for (int e = 0; e < 16; ++e) acc[n][e] = 0.f;
    v4u xf[2][4]; v2u wf[2][4][3][2];
    auto rdF = [&](int set, int st, int q) __attribute__((always_inline)) {
      LAS unsigned char* sb = sm + (st % 3) * STAGE;
      const int s = q / 7, e = q % 7;
      if (e == 0) xf[set][s] = *(LAS v4u*)(sb + (xa0 ^ (s * 32)));
      else wf[set][s][(e - 1) >> 1][(e - 1) & 1] = trread(sb + wa0 + s * 3072 + ((e - 1) & 1) * 768 + ((e - 1) >> 1) * 64);
    };
    auto wfrag = [&](int set, int s, int n) __attribute__((always_inline)) { v4u v; v.x = wf[set][s][n][0].x; v.y = wf[set][s][n][0].y; v.z = wf[set][s][n][1].x; v.w = wf[set][s][n][1].y; return v; };
    auto mmF = [&](int set, int m) __attribute__((always_inline)) { const int s = m / 3, n = m % 3; acc[n] = mfma32(wfrag(set, s, n), xf[set][s], acc[n]); };
    BAR();
    STAMP(9);
#pragma unroll
    for (int q = 0; q < 28; ++q) rdF(0, 0, q);
    SCHED();
#pragma unroll
    for (int m = 0; m < 12; ++m) { mmF(0, m); SCHED(); }
    BAR();
    const unsigned ones = 0x3f803f80u;
    {
      {
        LAS unsigned char* xp = sm + XROW + (2 * w + hh) * 1280;
        const v4u xa = *(LAS v4u*)(xp + t32 * 16), xb = *(LAS v4u*)(xp + 512 + t32 * 16); const v2u xc = *(LAS v2u*)(xp + 1024 + t32 * 8);
        const unsigned xw[10] = {xa.x, xa.y, xa.z, xa.w, xb.x, xb.y, xb.z, xb.w, xc.x, xc.y};
        f32x2 xv2[10], a2 = {0.f, 0.f};
#pragma unroll
        for (int k = 0; k < 10; ++k) { xv2[k] = f32x2{bf2f(xw[k] & 0xffffu), bfhi(xw[k])}; a2 += xv2[k]; }
        const float mean = sum32(a2[0] + a2[1]) * (1.f / D);
        f32x2 q2 = {0.f, 0.f};
#pragma unroll
        for (int k = 0; k < 10; ++k) { const f32x2 d0 = xv2[k] - mean; q2 += d0 * d0; }
        const float var = sum32(q2[0] + q2[1]) * (1.f / D);
        const f32x2 st = {__builtin_amdgcn_rcpf(rbf(__builtin_amdgcn_sqrtf(rbf(rbf(var) + 1e-5f)))), rbf(mean)};
        if (t32 == 0) __builtin_amdgcn_raw_buffer_store_b64(__builtin_bit_cast(v2u, st), rs, PRS_OFF + (r * 128 + 8 * j + 2 * w + hh) * 8, 0, AUX_SC1);
        SCHED();
      }
      {
        if (w < 3) {
          const int br = 2 * w + hh > 4 ? 4 : 2 * w + hh;
          LAS unsigned char* xp = sm + BROW + br * 1280;
          const v4u xa = *(LAS v4u*)(xp + t32 * 16), xb = *(LAS v4u*)(xp + 512 + t32 * 16); const v2u xc = *(LAS v2u*)(xp + 1024 + t32 * 8);
          const int gl = (lane >> 4) & 1, f16 = 3 * fu + w;
          v2u lo[3], hi[3];
#pragma unroll
          for (int q3 = 0; q3 < 3; ++q3) {
            const int nt = q3 < 2 ? 2 * q3 + hh : 4;
            LAS unsigned char* ra = sm + W2SCR + (16 * w + 4 * gl + ((lane & 15) >> 2)) * 160 + (16 * nt + 4 * (lane & 3)) * 2;
            lo[q3] = trread(ra); hi[q3] = trread(ra + 8 * 160);
          }
          const float c1 = sum32(dot2(xc.y, ones, dot2(xc.x, ones, sum8(xb, sum8(xa, 0.f)))));
          const int chn = 5 * (r & 7) + br, part = r >> 3;
          const int col = chn < 32 ? 32 * (chn >> 4) + 16 * part + (chn & 15) : 2 * chn + part;
          if (t32 == 0) __builtin_amdgcn_raw_buffer_store_b32(__builtin_bit_cast(unsigned, c1), rs, PCB_OFF + (j * 256 + col) * 4, 0, AUX_SC1);
#pragma unroll
          for (int q3 = 0; q3 < 3; ++q3) {
            const int nt = q3 < 2 ? 2 * q3 + hh : 4;
            v4u ov; ov.x = lo[q3].x; ov.y = lo[q3].y; ov.z = hi[q3].x; ov.w = hi[q3].y;
            __builtin_amdgcn_raw_buffer_store_b128(ov, rs, WT_OFF + ((ctu * 88 + 40 + (f16 >> 1)) * 5 + nt) * 1024 + (2 * (f16 & 1) + gl) * 256 + n16 * 16, 0, AUX_SC1);
          }
        } else {
          float cs6[6] = {0.f, 0.f, 0.f, 0.f, 0.f, 0.f};
#pragma unroll
          for (int m = 0; m < 3; ++m) {
            const int kq = m < 2 ? g + 4 * m : 8 + (g & 1);
            const unsigned wgt = (m < 2 || g < 2) ? ones : 0u;
            LAS unsigned char* ra = sm + W1SCR + (4 * kq + ((lane & 15) >> 2)) * 192 + 8 * (lane & 3);
#pragma unroll
            for (int c6 = 0; c6 < 6; ++c6) { const v2u v = trread(ra + c6 * 32); cs6[c6] = dot2(v.y, wgt, dot2(v.x, wgt, cs6[c6])); }
          }
#pragma unroll
          for (int c6 = 0; c6 < 6; ++c6) cs6[c6] = add32(add16(cs6[c6]));
          const float v0 = g == 0 ? cs6[0] : g == 1 ? cs6[1] : g == 2 ? cs6[2] : cs6[3], v1 = (g & 1) ? cs6[5] : cs6[4];
          const int po = PCF_OFF + (j * 16 + r) * 384;
          __builtin_amdgcn_raw_buffer_store_b32(__builtin_bit_cast(unsigned, v0), rs, po + lane * 4, 0, AUX_SC1);
          __builtin_amdgcn_raw_buffer_store_b32(__builtin_bit_cast(unsigned, v1), rs, po + 256 + t32 * 4, 0, AUX_SC1);
        }
        SCHED();
      }
      {
        LAS u16* raw = (LAS u16*)(sm + RAW);
        if (w == 0) {
          const int l40 = lane < 40 ? lane : 39;
          const float dt = rbf(__expf(bf2f(raw[80 + l40])));
          const float lr = bf2f(raw[l40]), li = bf2f(raw[40 + l40]);
          const float mag = __expf(dt * lr);
          float rev = dt * li * 0.15915494309189535f; rev -= floorf(rev);
          const float ar = mag * __builtin_amdgcn_cosf(rev), ai = mag * __builtin_amdgcn_sinf(rev);
          const float nr = ar - 1.f, den = __builtin_amdgcn_rcpf(lr * lr + li * li);
          const float cr = (nr * lr + ai * li) * den, ci = (ai * lr - nr * li) * den;
          float pr = ar, pi = ai;
#pragma unroll
          for (int s = 0; s < 3; ++s) { const float t = pr * pr - pi * pi; pi = 2.f * pr * pi; pr = t; }
          const float p8r = pr, p8i = pi;
#pragma unroll
          for (int s = 0; s < 4; ++s) { const float t = pr * pr - pi * pi; pi = 2.f * pr * pi; pr = t; }
          LAS float* pm = (LAS float*)(sm + PRM) + l40;
          pm[0] = ar; pm[40] = ai; pm[80] = p8r; pm[120] = p8i; pm[160] = pr; pm[200] = pi; pm[240] = cr; pm[280] = ci;
        } else if (w == 1) {
          const int l48 = lane < 48 ? lane : 47;
          const unsigned v = *(LAS unsigned*)(sm + RAW + 240 + l48 * 4);
          *(LAS f32x2*)(sm + B1F + l48 * 8) = f32x2{bf2f(v & 0xffffu), bfhi(v)};
        } else {
          const int l40 = lane < 40 ? lane : 39;
          const unsigned v = *(LAS unsigned*)(sm + RAW + 432 + (w - 2) * 160 + l40 * 4);
          *(LAS f32x2*)(sm + DB2 + (w - 2) * 320 + l40 * 8) = f32x2{bf2f(v & 0xffffu), bfhi(v)};
        }
        SCHED();
      }
    }
    TS(1); TS(2); BAR(); TS(3);
#pragma unroll
    for (int q = 0; q < 28; ++q) rdF(1, 1, q);
    WAITL0(); BAR();
    const u64 tag = read_tag();
    v4u bw[DBF][5];
    const int boff = WT_OFF + (ct * 88 + 40 + 12 * w) * 5120 + lane * 16;
    const int s2 = s2f;
    auto stepF = [&](auto I_) __attribute__((always_inline)) {
      constexpr int i = decltype(I_)::value;
#pragma unroll
      for (int m = 0; m < 8; ++m) {
        mmF(i & 1, m);
        if (i < 9) {
#pragma unroll
          for (int q = (m < 4 ? 4 * m : 3 * m + 4); q < (m < 4 ? 4 * m + 4 : 3 * m + 7); ++q) rdF((i + 1) & 1, i + 1, q);
        }
        SCHED();
      }
      if (i < 9) {
        WAITL0();
        TS(1 + 3 * i);
        if (i == 2) waitv<0>();
        TS(2 + 3 * i);
        BAR();
        TS(3 + 3 * i);
      }
      SCHED();
#pragma unroll
      for (int m = 8; m < 12; ++m) {
        mmF(i & 1, m);
        if constexpr (i >= 8) {
          bw[i - 8][m - 8] = ldsc1(rs, boff + (m - 8) * 1024, rotk<12>(i - 8, s2) * 5120);
          if (m == 11) bw[i - 8][4] = ldsc1(rs, boff + 4096, rotk<12>(i - 8, s2) * 5120);
        }
        SCHED();
      }
    };
    stepF(IC(1)); stepF(IC(2)); stepF(IC(3)); stepF(IC(4)); stepF(IC(5)); stepF(IC(6)); stepF(IC(7)); stepF(IC(8)); stepF(IC(9));
    STAMP(1); TQ(0);
    {
      lds_wait(cnt + 8, 1u);
      const f32x2 st2 = *(LAS f32x2*)(sm + ST2 + (32 * w + t32) * 8);
      f32x4 S4[12], b4[12];
#pragma unroll
      for (int k = 0; k < 12; ++k) { S4[k] = *(LAS f32x4*)(sm + CSF + (8 * k + 4 * hh) * 4); b4[k] = *(LAS f32x4*)(sm + B1F + (8 * k + 4 * hh) * 4); }
      const float rsd = st2[0], nm = -rsd * st2[1];
      const int hoff = H1T_OFF + (((2 * r + (w >> 1)) * 48 + 3 * j) * 4 + 2 * (w & 1) + (t32 >> 4)) * 1024 + hh * 256 + (t32 & 15) * 16;
#pragma unroll
      for (int n = 0; n < 3; ++n) {
        unsigned ou[8];
#pragma unroll
        for (int e = 0; e < 8; ++e) {
          const f32x2 a = {acc[n][2 * e], acc[n][2 * e + 1]};
          const f32x4 S = S4[4 * n + (e >> 1)], bb = b4[4 * n + (e >> 1)];
          const f32x2 S2 = {S[2 * (e & 1)], S[2 * (e & 1) + 1]}, b2_ = {bb[2 * (e & 1)], bb[2 * (e & 1) + 1]};
          const f32x2 v = rsd * a + (nm * S2 + b2_);
          const f32x2 z = v * (-2.302208198f - 0.1029432397f * (v * v));
          f32x2 d = {__builtin_amdgcn_exp2f(z[0]), __builtin_amdgcn_exp2f(z[1])};
          d += 1.f;
          const f32x2 rc = {__builtin_amdgcn_rcpf(d[0]), __builtin_amdgcn_rcpf(d[1])};
          const f32x2 gl = v * rc;
          ou[e] = pk2(gl[0], gl[1]);
        }
        __builtin_amdgcn_raw_buffer_store_b128(v4u{ou[0], ou[1], ou[2], ou[3]}, rs, hoff + n * 4096, 0, AUX_SC1);
        __builtin_amdgcn_raw_buffer_store_b128(v4u{ou[4], ou[5], ou[6], ou[7]}, rs, hoff + n * 4096 + 512, 0, AUX_SC1);
        SCHED();
        TQ(1 + n);
      }
    }
    STAMP(4);
    v2u xv[5];
    { const u16* xp = x + (size_t)(128 * r + 64 * half2 + 16 * w + n16) * D + 80 * ct + 4 * g;
#pragma unroll
      for (int n = 0; n < 5; ++n) xv[n] = *(const v2u*)(xp + 16 * n); }
    SCHED();
    waitv<5>();
    if (lane == 0) __hip_atomic_store((GAS u64*)(ws + FLAG_F_OFF) + ((r * 2 + (w >> 1)) * 16 + j) * 2 + (w & 1), tag, RLX_AGENT);
    STAMP(5); TQ(4);
#pragma unroll
    for (int k = PBF; k < DBF; ++k)
#pragma unroll
      for (int n = 0; n < 5; ++n) bw[k][n] = ldsc1(rs, boff + n * 1024, rotk<12>(k, s2) * 5120);
    const int trow = 64 * half2 + 16 * w + n16;
    {
      float est[2] = {((LAS float*)(sm + ST2))[2 * trow], ((LAS float*)(sm + ST2))[2 * trow + 1]};
      asm volatile("" : "+v"(est[0])); asm volatile("" : "+v"(est[1]));
#pragma unroll
      for (int n = 0; n < 5; ++n) {
        const f32x4 ds = *(LAS f32x4*)(sm + DB2 + (16 * n + 4 * g) * 4);
        const unsigned d0 = pk2(bf2f(xv[n].x & 0xffffu) - est[1], bfhi(xv[n].x) - est[1]), d1 = pk2(bf2f(xv[n].y & 0xffffu) - est[1], bfhi(xv[n].y) - est[1]);
        const unsigned n0 = pk2(bf2f(d0 & 0xffffu) * est[0], bfhi(d0) * est[0]), n1 = pk2(bf2f(d1 & 0xffffu) * est[0], bfhi(d1) * est[0]);
        *(LAS v2u*)(sm + SKR + w * 2560 + n * 512 + lane * 8) = v2u{pk2(bf2f(n0 & 0xffffu) * ds[0], bfhi(n0) * ds[1]), pk2(bf2f(n1 & 0xffffu) * ds[2], bfhi(n1) * ds[3])};
      }
    }
    TQ(5);
    poll_flags((const GAS u64*)(ws + FLAG_F_OFF) + ((r * 2 + half2) * 16 + 4 * w) * 2 + (lane & 7), tag);
    STAMP(6); TQ(6); TG(1);
    f32x4 ac2[5][4];
#pragma unroll
    for (int n = 0; n < 5; ++n)
#pragma unroll
      for (int i = 0; i < 4; ++i) ac2[n][i] = f32x4{0.f, 0.f, 0.f, 0.f};
    int aoff[4];
#pragma unroll
    for (int i = 0; i < 4; ++i) aoff[i] = H1T_OFF + ((rt64 * 48 + 12 * w) * 4 + ((i + w) & 3)) * 1024 + lane * 16;
    __builtin_amdgcn_s_setprio(1);
    stream_gemm<12, DBF, DAF, NLF>(rs, boff, aoff, s2, bw, ac2,
        [&](int k) __attribute__((always_inline)) { if (k == 0) lds_wait(cnt + 4 + w, 1u); return sm + fslot(w, k) + lane * 16; },
        [&]() __attribute__((always_inline)) {}, [&](int k) __attribute__((always_inline)) { TG(2 + k); });
    STAMP(7); TQ(7);
#pragma unroll
    for (int i = 1; i < 4; ++i)
#pragma unroll
      for (int n = 0; n < 5; ++n) *(LAS f32x4*)(sm + fslot(w, i - 1) + n * 1024 + lane * 16) = ac2[n][i];
    lds_signal(cnt + 2, lane);
    v2u gs[5];
    TQ(8);
    lds_wait(cnt + 2, 4u);
    TQ(9);
    f32x4 h2[5];
#pragma unroll
    for (int n = 0; n < 5; ++n) {
      h2[n] = ac2[n][0];
#pragma unroll
      for (int o = 1; o < 4; ++o) h2[n] += *(LAS f32x4*)(sm + fslot((w + o) & 3, 3 - o) + n * 1024 + lane * 16);
    }
    lds_signal(cnt + 9, lane);
#pragma unroll
    for (int n = 0; n < 5; ++n) {
      const f32x4 bs = *(LAS f32x4*)(sm + DB2 + 320 + (16 * n + 4 * g) * 4);
      const unsigned r0 = pk2(h2[n][0], h2[n][1]), r1 = pk2(h2[n][2], h2[n][3]);
      const unsigned a0 = pk2(bf2f(r0 & 0xffffu) + bs[0], bfhi(r0) + bs[1]), a1 = pk2(bf2f(r1 & 0xffffu) + bs[2], bfhi(r1) + bs[3]);
      const float a[4] = {bf2f(a0 & 0xffffu), bfhi(a0), bf2f(a1 & 0xffffu), bfhi(a1)};
      float sg[4];
#pragma unroll
      for (int e = 0; e < 4; ++e) sg[e] = a[e] * __builtin_amdgcn_rcpf(1.f + __builtin_amdgcn_exp2f(-1.4426950408889634f * a[e]));
      gs[n].x = pk2(sg[0], sg[1]); gs[n].y = pk2(sg[2], sg[3]);
      asm volatile("" : "+v"(gs[n]));
    }
    STAMP(2); TQ(10);
    lds_wait(cnt + 9, 4u);
#pragma unroll
    for (int n = 0; n < 5; ++n) *(LAS v2u*)(sm + fslot(w, 0) + n * 512 + lane * 8) = gs[n];
    lds_signal(cnt + 10 + w, lane);
    STAMP(8); TQ(11);
    TS_OUT();
  }
}
}

extern "C" void kernel_launch(void* const* d_in, const int* in_sizes, int n_in,
                              void* d_out, int out_size, void* d_ws, size_t ws_size,
                              hipStream_t stream) {
  using s5k::u16;
  (void)hipFuncSetAttribute((const void*)s5k::s5_fused, hipFuncAttributeMaxDynamicSharedMemorySize, s5k::LDS_BYTES);
  s5k::s5_fused<<<s5k::NWG, s5k::NTHR, s5k::LDS_BYTES, stream>>>(
      (const u16*)d_in[0], (const u16*)d_in[1], (const u16*)d_in[2], (const u16*)d_in[3], (const u16*)d_in[4], (const u16*)d_in[5],
      (const u16*)d_in[6], (const u16*)d_in[7], (const u16*)d_in[8], (const u16*)d_in[10], (const u16*)d_in[11], (const u16*)d_in[12],
      (const u16*)d_in[13], (float*)d_out, (unsigned char*)d_ws);
}
```

```cpp
#include <hip/hip_runtime.h>
#include <hip/hip_bf16.h>
#include <stdint.h>
#include <type_traits>

namespace s5k {
typedef unsigned short u16;
typedef unsigned long long u64;
typedef unsigned v4u __attribute__((ext_vector_type(4)));
typedef unsigned v2u __attribute__((ext_vector_type(2)));
typedef float f32x4 __attribute__((ext_vector_type(4)));
typedef float f32x2 __attribute__((ext_vector_type(2)));
typedef float f32x16 __attribute__((ext_vector_type(16)));
typedef __bf16 bf16x8 __attribute__((ext_vector_type(8)));
typedef __bf16 bf16x2 __attribute__((ext_vector_type(2)));
typedef short s16x4 __attribute__((ext_vector_type(4)));
#define WAVE_ALL(c) (__builtin_amdgcn_ballot_w64(!(c)) == 0ull)
#define GAS __attribute__((address_space(1)))
#define LAS __attribute__((address_space(3)))
#define RLX_AGENT __ATOMIC_RELAXED, __HIP_MEMORY_SCOPE_AGENT
#define RLX_WG __ATOMIC_RELAXED, __HIP_MEMORY_SCOPE_WORKGROUP

constexpr int L = 2048, D = 640, P = 640, F = 1536;
constexpr int NWG = 256, NTHR = 512;
constexpr int EPOCH_OFF = 0;
constexpr int FLAG_E_OFF = 4096;
constexpr int FLAG_F_OFF = 65536;
constexpr int FLAG_H_OFF = 65536 + 8192;
constexpr int EG_OFF = 131072;
constexpr int PRS_OFF = 512 << 10;
constexpr int PCB_OFF = 576 << 10;
constexpr int PCF_OFF = 640 << 10;
constexpr int WT_OFF = 1 << 20;
constexpr int H1T_OFF = 8 << 20;
constexpr int HT_OFF = 16 << 20;
constexpr int WS_BYTES = 24 << 20;
constexpr int XS = 0, BWS = 16384, W1S = 26624, STAGE = 38912;
constexpr int XR = 3 * STAGE;
constexpr int XROW = XR, BROW = XR + 10240, W1SCR = BROW + 6400, W2SCR = W1SCR + 7680, CSCR = W2SCR + 7680, RAW = CSCR + 8192;
constexpr int MISC = 158976;
static_assert(RAW + 1024 <= MISC, "");
constexpr int ST2 = MISC;
constexpr int CSB = MISC + 1024;
constexpr int CSF = MISC + 2048;
constexpr int B1F = MISC + 2432;
constexpr int PRM = MISC + 2816;
constexpr int CNT = MISC + 4096;
constexpr int DB2 = MISC + 4160;
constexpr int HIN = CSB + 384;
constexpr int LDS_BYTES = MISC + 4864;
static_assert(HIN + 640 <= LDS_BYTES, "");
constexpr int SLOT0 = 40960;
constexpr int ESR = SLOT0 + 20 * 5120;
__device__ __forceinline__ int yslot(int w, int k) { return SLOT0 + (k ? 11 + w : (w == 3 ? 19 : 4 + w)) * 5120; }
__device__ __forceinline__ int fslot(int w, int k) { return SLOT0 + (k == 0 ? 15 + w : k == 1 ? w : 7 + w) * 5120; }
constexpr int SKR = ESR + 5120;
constexpr int JUNK = SKR + 10240;
static_assert(JUNK + 256 <= MISC, "");
static_assert(LDS_BYTES <= 163840, "");
constexpr unsigned SPIN_CAP = 1u << 16;
constexpr int AUX_SC1 = 16;

__device__ __forceinline__ float bf2f(unsigned b) { return __builtin_bit_cast(float, b << 16); }
__device__ __forceinline__ float bfhi(unsigned b) { return __builtin_bit_cast(float, b & 0xffff0000u); }
__device__ __forceinline__ unsigned pk2(float a, float b) { f32x2 f = {a, b}; return __builtin_bit_cast(unsigned, __builtin_convertvector(f, bf16x2)); }
__device__ __forceinline__ float rbf(float a) { return bf2f(pk2(a, a) & 0xffffu); }
__device__ __forceinline__ float dot2(unsigned a, unsigned b, float c) {
  return __builtin_amdgcn_fdot2_f32_bf16(__builtin_bit_cast(bf16x2, a), __builtin_bit_cast(bf16x2, b), c, false);
}
__device__ __forceinline__ float sum8(v4u a, float c) {
  const unsigned ones = 0x3f803f80u;
  c = dot2(a.x, ones, c); c = dot2(a.y, ones, c); c = dot2(a.z, ones, c); c = dot2(a.w, ones, c); return c;
}
__device__ __forceinline__ float swp1(float v) {
  return __builtin_bit_cast(float, __builtin_amdgcn_update_dpp(0, __builtin_bit_cast(int, v), 0xB1, 0xF, 0xF, true));
}
template <int CTRL> __device__ __forceinline__ float dppf(float v) {
  return __builtin_bit_cast(float, __builtin_amdgcn_update_dpp(0, __builtin_bit_cast(int, v), CTRL, 0xF, 0xF, true));
}
__device__ __forceinline__ float add16(float v) {
  float c_ = v;
  asm("s_nop 1\n\tv_permlane16_swap_b32 %0, %1" : "+v"(v), "+v"(c_));
  return v + c_;
}
__device__ __forceinline__ float add32(float v) {
  float c_ = v;
  asm("s_nop 1\n\tv_permlane32_swap_b32 %0, %1" : "+v"(v), "+v"(c_));
  return v + c_;
}
__device__ __forceinline__ float sum32(float v) {
  v += dppf<0xB1>(v); v += dppf<0x4E>(v); v += dppf<0x141>(v); v += dppf<0x140>(v); return add16(v);
}
__device__ __forceinline__ f32x4 mfma16(v4u a, v4u b, f32x4 c) {
  return __builtin_amdgcn_mfma_f32_16x16x32_bf16(__builtin_bit_cast(bf16x8, a), __builtin_bit_cast(bf16x8, b), c, 0, 0, 0);
}
__device__ __forceinline__ f32x16 mfma32(v4u a, v4u b, f32x16 c) {
  return __builtin_amdgcn_mfma_f32_32x32x16_bf16(__builtin_bit_cast(bf16x8, a), __builtin_bit_cast(bf16x8, b), c, 0, 0, 0);
}
__device__ __forceinline__ v2u trread(LAS unsigned char* p) {
  return __builtin_bit_cast(v2u, __builtin_amdgcn_ds_read_tr16_b64_v4i16((LAS s16x4*)p));
}
__device__ __forceinline__ void dma1(const void* gb, unsigned vo, unsigned ld) {
  asm volatile("s_mov_b32 m0, %2\n\ts_nop 0\n\tglobal_load_lds_dwordx4 %1, %0" :: "s"(gb), "v"(vo), "s"(ld) : "memory");
}
__device__ __forceinline__ void dma1h(const void* gb, unsigned vo, unsigned ld) {
  asm volatile("s_mov_b32 m0, %2\n\ts_mov_b32 exec_hi, 0\n\tglobal_load_lds_dwordx4 %1, %0\n\ts_mov_b32 exec_hi, -1" :: "s"(gb), "v"(vo), "s"(ld) : "memory");
}
__device__ __forceinline__ void dma1s(const void* gb, unsigned vo, unsigned ld) {
  asm volatile("s_mov_b32 m0, %2\n\ts_nop 0\n\tglobal_load_lds_dwordx4 %1, %0 sc1" :: "s"(gb), "v"(vo), "s"(ld) : "memory");
}
template <int N> __device__ __forceinline__ void waitv() { asm volatile("s_waitcnt vmcnt(%0)" :: "i"(N) : "memory"); }
#define SCHED() __builtin_amdgcn_sched_barrier(0)
#define WAITL0() asm volatile("s_waitcnt lgkmcnt(0)" ::: "memory")
#define BAR() __builtin_amdgcn_s_barrier()

__device__ __forceinline__ u64 mix64(u64 z) {
  z += 0x9E3779B97F4A7C15ull; z = (z ^ (z >> 30)) * 0xBF58476D1CE4E5B9ull; z = (z ^ (z >> 27)) * 0x94D049BB133111EBull; return z ^ (z >> 31);
}
__device__ __forceinline__ void poll_flags(const GAS u64* f, u64 tag) {
#pragma nounroll
  for (unsigned spins = 0; spins < SPIN_CAP; ++spins) {
    const u64 v = __hip_atomic_load(f, RLX_AGENT);
    if (WAVE_ALL(v == tag)) break;
    __builtin_amdgcn_s_sleep(1);
  }
  __builtin_amdgcn_fence(__ATOMIC_ACQUIRE, "wavefront");
}
__device__ __forceinline__ void lds_signal(LAS unsigned* c, int lane) {
  WAITL0();
  if (lane == 0) __hip_atomic_fetch_add(c, 1u, RLX_WG);
}
__device__ __forceinline__ void lds_wait(LAS unsigned* c, unsigned want) {
#pragma nounroll
  for (unsigned spins = 0; spins < SPIN_CAP * 16; ++spins) {
    if (__hip_atomic_load(c, RLX_WG) >= want) break;
    __builtin_amdgcn_s_sleep(1);
  }
  asm volatile("" ::: "memory");
}
template <int NK> __device__ __forceinline__ int rotk(int k, int s) { const int a = k + s; return a >= NK ? a - NK : a; }
__device__ __forceinline__ v4u ldsc1(__amdgpu_buffer_rsrc_t rs, int voff, int soff) { return __builtin_amdgcn_raw_buffer_load_b128(rs, voff, soff, AUX_SC1); }

template <int NK, int DB, int DA, int NL, class LW, class HK>
__device__ __forceinline__ void stream_gemm(__amdgpu_buffer_rsrc_t rs, int boff, const int (&aoff)[4], int s2, v4u (&bw)[DB][5], f32x4 (&acc)[5][4], LW lw, HK hook) {
  v4u aw[DA][4];
#pragma unroll
  for (int k = 0; k < DA; ++k) {
#pragma unroll
    for (int i = 0; i < 4; ++i) aw[k][i] = ldsc1(rs, aoff[i], rotk<NK>(k, s2) * 4096);
    SCHED();
  }
#pragma unroll
  for (int k = 0; k < NK; ++k) {
#pragma unroll
    for (int i = 0; i < 4; ++i) {
#pragma unroll
      for (int n = 0; n < 5; ++n) acc[n][i] = mfma16(bw[k % DB][n], aw[k % DA][i], acc[n][i]);
      SCHED();
      if (k + DA < NK) aw[k % DA][i] = ldsc1(rs, aoff[i], rotk<NK>(k + DA, s2) * 4096);
      SCHED();
    }
    if (k == NK - DA) hook();
    if (k + DB < NK) {
      if (k < NL) {
        LAS unsigned char* lp = lw(k);
#pragma unroll
        for (int n = 0; n < 5; ++n) bw[k % DB][n] = *(LAS v4u*)(lp + n * 1024);
      } else {
#pragma unroll
        for (int n = 0; n < 5; ++n) bw[k % DB][n] = ldsc1(rs, boff + n * 1024, rotk<NK>(k + DB, s2) * 5120);
      }
    }
    SCHED();
  }
}

#define ROT1 2
#define ROT2 0
#define DAF_ 3
#define DAY_ 3
constexpr int DBF = 5, DAF = DAF_, DBY = 5, DAY = DAY_, NLF = 3, NLY = 2;
constexpr int PBF = 2, PBY = 3;
__device__ __forceinline__ void dma1m(const void* gb, unsigned vo, unsigned ld, int sh) {
  asm volatile("s_mov_b32 m0, %2\n\ts_lshr_b64 exec, exec, %3\n\tglobal_load_lds_dwordx4 %1, %0\n\ts_mov_b64 exec, -1" :: "s"(gb), "v"(vo), "s"(ld), "s"(sh) : "memory", "scc");
}
constexpr int CODE_LINES = 344;
#define IC(n) std::integral_constant<int, n>{}

#if defined(__HIP_DEVICE_COMPILE__) && !defined(USE_PK)
#define NOPK_ATTR __attribute__((target("no-packed-fp32-ops")))
#else
#define NOPK_ATTR
#endif
__global__ __launch_bounds__(NTHR, 2) NOPK_ATTR void s5_fused(const u16* __restrict__ x, const u16* __restrict__ lre, const u16* __restrict__ lim,
    const u16* __restrict__ Bre, const u16* __restrict__ Bim, const u16* __restrict__ Cre, const u16* __restrict__ Cim,
    const u16* __restrict__ logdt, const u16* __restrict__ Dsk, const u16* __restrict__ w1, const u16* __restrict__ b1,
    const u16* __restrict__ w2, const u16* __restrict__ b2, float* __restrict__ out, unsigned char* __restrict__ ws) {
  extern __shared__ __attribute__((aligned(16))) unsigned char smem_[];
  LAS unsigned char* sm = (LAS unsigned char*)smem_;
  asm volatile("" :: "s"(x), "s"(lre), "s"(lim), "s"(Bre), "s"(Bim), "s"(Cre), "s"(Cim), "s"(logdt), "s"(Dsk), "s"(w1), "s"(b1), "s"(w2), "s"(b2), "s"(out), "s"(ws));
  u64 pc0; asm volatile("s_getpc_b64 %0" : "=s"(pc0));
  const int tid = threadIdx.x, lane = tid & 63;
  const int wv = __builtin_amdgcn_readfirstlane(tid >> 6), w = wv & 3;
  const int b = blockIdx.x, xcd = b & 7, idx = b >> 3;
  const int r = 4 * (xcd >> 1) + (idx & 3), jj = idx >> 2, j = 8 * (xcd & 1) + jj;
  const int half2 = jj & 1, ct = 4 * (xcd & 1) + (jj >> 1), rt64 = 2 * r + half2;
  const int s1 = ROT1 == 1 ? (j + 3 * r) % 10 : ROT1 == 2 ? 5 * ((j + r) & 1) : ROT1 == 3 ? 3 * ((j + r) % 3) : 0;
  const unsigned smb = (unsigned)(size_t)sm;
  const __amdgpu_buffer_rsrc_t rs = __builtin_amdgcn_make_buffer_rsrc((void*)ws, 0, WS_BYTES, 0x00020000);
  LAS unsigned* cnt = (LAS unsigned*)(sm + CNT);
  const int n16 = lane & 15, g = lane >> 4;
  const int s2f = ROT2 ? 3 * (ct & 3) : 0, s2y = ROT2 ? (5 * (ct & 3)) >> 1 : 0;
  const int ctu = b & 7, fu = b >> 3;
  auto read_tag = [&]() __attribute__((always_inline)) {
    const v2u e = *(LAS v2u*)(sm + RAW + 47 * 16);
    return mix64(((u64)__builtin_amdgcn_readfirstlane(e.y) << 32) | (unsigned)__builtin_amdgcn_readfirstlane(e.x));
  };

  if (__builtin_expect(wv < 4, 1)) {
    unsigned so[10];
    { const int xrow = 32 * w + (lane >> 3);
      const unsigned xb = (unsigned)(128 * r + xrow) * 1280u + (((lane & 7) ^ ((xrow >> 1) & 1)) << 4);
#pragma unroll
      for (int q = 0; q < 4; ++q) so[q] = (xb + q * 10240u) ^ (q << 5); }
    const unsigned char* bmat = (const unsigned char*)((w >> 1) ? Bim : Bre);
    const unsigned l16 = lane * 16;
    auto job = [&](int d) __attribute__((always_inline)) {
      if (d < 100) {
        const int st = d / 10, p = d % 10, kt = rotk<10>(st, s1);
        const unsigned sb = smb + (st % 3) * STAGE;
        if (p < 4) dma1((const unsigned char*)x + kt * 128, so[p], sb + XS + (32 * w + 8 * p) * 128);
        else if (p < 6) dma1(bmat + kt * 128, so[p], sb + BWS + 2560 * w + (p - 4) * 1024);
        else if (p == 6) dma1h(bmat + kt * 128, so[p], sb + BWS + 2560 * w + 2048);
        else dma1((const unsigned char*)w1 + (size_t)kt * (64 * 3072), so[p], sb + W1S + 3072 * w + (p - 7) * 1024);
      } else if (d < 102) {
        const int pc = 2 * w + (d - 100);
        dma1s(ws + (pc == 0 ? PRS_OFF + r * 1024 : pc == 1 ? PCB_OFF + j * 1024 : PCF_OFF + j * 6144 + (pc - 2) * 1024), l16,
              smb + (pc == 0 ? ST2 : pc == 1 ? CSB : ESR + (pc - 2) * 1024));
      } else if (d < 127) {
        const int sl = (d - 102) / 5, n = (d - 102) % 5;
        if (sl == 2 || sl == 4) dma1s(ws + WT_OFF + (ct * 88 + 10 * w + rotk<10>(DBY + (sl >> 2), s2y)) * 5120 + n * 1024, l16, smb + yslot(w, sl >> 2) + n * 1024);
        else { const int k = sl == 3 ? 2 : sl; dma1s(ws + WT_OFF + (ct * 88 + 40 + 12 * w + rotk<12>(DBF + k, s2f)) * 5120 + n * 1024, l16, smb + fslot(w, k) + n * 1024); }
      }
    };
#pragma unroll
    for (int d = 0; d < 4; ++d) job(d);
#pragma unroll
    for (int q3 = 0; q3 < 3; ++q3) {
      int ch = 64 * q3 + lane; if (ch > 159) ch = 159;
      const int cl = 20 * (w & 1) + (ch >> 3), cs_ = ch & 7;
      so[4 + q3] = (unsigned)(40 * j + cl) * 1280u + ((cs_ ^ ((cl >> 1) & 7) ^ ((w >> 1) << 2)) << 4);
      const int c2 = 192 * w + 64 * q3 + lane;
      so[7 + q3] = (unsigned)(c2 / 12) * 3072u + j * 192 + (c2 % 12) * 16;
    }
#pragma unroll
    for (int d = 4; d < 10; ++d) job(d);
    if (tid < 16) cnt[tid] = 0u;
    const unsigned o1 = (unsigned)(40 * r + lane / 12) * 3072u + j * 192 + (lane % 12) * 16;
    const unsigned o2 = (unsigned)(48 * fu + 12 * w + lane / 10) * 1280u + ctu * 160 + (lane % 10) * 16;
    const int u = b * 4 + w, cu = u < 800 ? u : u - 800, cd8 = cu / 10, ccb = cu % 10;
    const unsigned o3 = (unsigned)((8 * cd8 + (lane >> 3)) * P + 64 * ccb + 8 * (lane & 7)) * 2;
    auto jobG2 = [&](int q) __attribute__((always_inline)) {
      const int p = 7 * w + q;
      if (p < 25) {
        const unsigned char* gb = p < 10 ? (const unsigned char*)x + (size_t)(128 * r + 8 * j) * 1280 + p * 1024
                                : p < 17 ? (const unsigned char*)(r < 8 ? Bre : Bim) + (size_t)(40 * j + 5 * (r & 7)) * 1280 + (p - 10) * 1024
                                         : (const unsigned char*)w1 + (p - 17) * (5 * 3072);
        dma1m(gb, p < 17 ? l16 : o1, smb + (p < 17 ? XROW + p * 1024 : W1SCR + (p - 17) * 960), p < 16 ? 0 : p == 16 ? 48 : 4);
      } else if (p == 25) {
        const u16* vb = lane < 5 ? lre + 40 * j + 8 * lane : lane < 10 ? lim + 40 * j + 8 * (lane - 5) : lane < 15 ? logdt + 40 * j + 8 * (lane - 10)
                      : lane < 27 ? b1 + 96 * j + 8 * (lane - 15) : lane < 37 ? Dsk + 80 * ct + 8 * (lane - 27) : lane < 47 ? b2 + 80 * ct + 8 * (lane - 37) : (const u16*)(ws + EPOCH_OFF);
        asm volatile("s_mov_b32 m0, %1\n\ts_nop 0\n\tglobal_load_lds_dwordx4 %0, off sc1" :: "v"(vb), "s"(smb + RAW) : "memory");
      }
    };
    auto jobG1 = [&](int q) __attribute__((always_inline)) {
      if (q < 2) dma1m((const unsigned char*)w2 + q * (6 * 1280), o2, smb + W2SCR + (2 * w + q) * 960, 4);
      else dma1(q == 2 ? Cre : Cim, o3, smb + CSCR + w * 2048 + (q - 2) * 1024);
    };
    const int rho = n16;
    const int Rrow = 32 * w + 8 * (rho >> 2) + (rho & 3);
    const int fx = ((rho >> 2) << 1) | ((rho >> 1) & 1);
    const int xa0 = XS + Rrow * 128 + ((g ^ fx) << 4);
    const int wb0 = BWS + rho * 128 + ((g ^ ((rho >> 1) & 7)) << 4);
    const int wb4 = BWS + (40 * (rho & 1) + 32 + (rho >> 1)) * 128 + ((g ^ (rho >> 2) ^ ((rho & 1) << 2)) << 4);
    f32x4 acc[2][5];
#pragma unroll
    for (int i = 0; i < 2; ++i)
#pragma unroll
      for (int t = 0; t < 5; ++t) acc[i][t] = f32x4{0.f, 0.f, 0.f, 0.f};
    v4u fa[2][2][2], fb[2][2][5];
    auto rdB = [&](int set, int st, int q) __attribute__((always_inline)) {
      LAS unsigned char* sb = sm + (st % 3) * STAGE;
      const int kk = q / 7, e = q % 7;
      if (e < 2) fa[set][kk][e] = *(LAS v4u*)(sb + (xa0 ^ (kk * 64)) + e * 512);
      else if (e < 6) { const int t = e - 2; fb[set][kk][t] = *(LAS v4u*)(sb + (wb0 ^ ((kk ^ (t & 1)) * 64)) + (t & 1) * 5120 + (t >> 1) * 2048); }
      else fb[set][kk][4] = *(LAS v4u*)(sb + (wb4 ^ (kk * 64)));
    };
    auto mmB = [&](int set, int m) __attribute__((always_inline)) {
      const int kk = m / 10, t = (m % 10) >> 1, i = m & 1;
      acc[i][t] = mfma16(fa[set][kk][i], fb[set][kk][t], acc[i][t]);
    };
    jobG2(0); jobG2(1);
    waitv<2>();
    BAR();
#pragma unroll
    for (int q = 2; q < 7; ++q) jobG2(q);
#pragma unroll
    for (int q = 0; q < 4; ++q) jobG1(q);
#pragma unroll
    for (int q = 0; q < 14; ++q) rdB(0, 0, q);
    SCHED();
#pragma unroll
    for (int m = 0; m < 20; ++m) { mmB(0, m); if (m & 1) job(10 + (m >> 1)); SCHED(); }
    waitv<10>(); BAR();
#pragma unroll
    for (int d = 20; d < 30; ++d) job(d);
    {
      LAS unsigned char* sc = sm + CSCR + w * 2048 + (lane & 7) * 128 + (lane >> 3) * 8;
      const v2u r0 = *(LAS v2u*)sc, i0 = *(LAS v2u*)(sc + 1024), r1 = *(LAS v2u*)(sc + 64), i1 = *(LAS v2u*)(sc + 1088);
      v4u o0, o1_;
      o0.x = __builtin_amdgcn_perm(i0.x, r0.x, 0x05040100u); o0.y = __builtin_amdgcn_perm(i0.x, r0.x, 0x07060302u);
      o0.z = __builtin_amdgcn_perm(i0.y, r0.y, 0x05040100u); o0.w = __builtin_amdgcn_perm(i0.y, r0.y, 0x07060302u);
      o1_.x = __builtin_amdgcn_perm(i1.x, r1.x, 0x05040100u); o1_.y = __builtin_amdgcn_perm(i1.x, r1.x, 0x07060302u);
      o1_.z = __builtin_amdgcn_perm(i1.y, r1.y, 0x05040100u); o1_.w = __builtin_amdgcn_perm(i1.y, r1.y, 0x07060302u);
      const int ctc = cd8 / 10, ntc = (cd8 % 10) >> 1;
      const int fo = WT_OFF + ((ctc * 88 + 4 * ccb + (lane >> 5)) * 5 + ntc) * 1024 + ((lane >> 3) & 3) * 256 + (8 * (cd8 & 1) + (lane & 7)) * 16;
      __builtin_amdgcn_raw_buffer_store_b128(o0, rs, fo, 0, AUX_SC1);
      __builtin_amdgcn_raw_buffer_store_b128(o1_, rs, fo + 2 * 5120, 0, AUX_SC1);
      SCHED();
    }
#pragma unroll
    for (int d = 30; d < 40; ++d) job(d);
    waitv<22>(); BAR();
#pragma unroll
    for (int q = 0; q < 14; ++q) rdB(1, 1, q);
    WAITL0(); waitv<12>(); BAR();
#pragma unroll
    for (int d = 40; d < 44; ++d) job(d);
    const u64 tag = read_tag();
    v4u efv[2];
    const int boff = WT_OFF + (ct * 88 + 10 * w) * 5120 + lane * 16;
    const int s2 = s2y;
    v4u bw[DBY][5];
    const int q = g, c = n16;
    LAS float* pm = (LAS float*)(sm + PRM);
    f32x4 st0, st1, st2_, st3; float Sc[5]; f32x2 lr2, li2; float lr4, ls4;
    auto stepB = [&](auto I_) __attribute__((always_inline)) {
      constexpr int i = decltype(I_)::value;
      auto jobA = [&](int a) __attribute__((always_inline)) {
        if (i <= 6) { if (a < 6) job(10 * (i + 3) + 4 + a); }
        else if (i == 7) job(106 + a);
        else if (i == 8) job(119 + a);
        else if (a < 5) bw[1][a] = ldsc1(rs, boff + a * 1024, rotk<10>(1, s2) * 5120);
      };
      auto jobB = [&](int a) __attribute__((always_inline)) {
        if (i <= 5) { if (a < 4) job(10 * (i + 4) + a); }
        else if (i == 6) job(100 + a);
        else if (i == 7) job(113 + a);
        else if (i == 8) { if (a == 0) job(126); else bw[0][a - 1] = ldsc1(rs, boff + (a - 1) * 1024, s2 * 5120); }
        else if (a < 5) bw[2][a] = ldsc1(rs, boff + a * 1024, rotk<10>(2, s2) * 5120);
      };
      if (i == 9) {
        LAS f32x4* sp = (LAS f32x4*)(sm + ST2 + (32 * w + 8 * q) * 8);
        st0 = sp[0]; st1 = sp[1]; st2_ = sp[2]; st3 = sp[3];
#pragma unroll
        for (int t = 0; t < 5; ++t) Sc[t] = ((LAS float*)(sm + CSB))[16 * t + c];
        lr2 = f32x2{pm[c], pm[16 + c]}; li2 = f32x2{pm[40 + c], pm[56 + c]};
        lr4 = pm[32 + (c >> 1)]; ls4 = pm[72 + (c >> 1)];
        SCHED();
      }
#pragma unroll
      for (int m = 0; m < 14; ++m) { mmB(i & 1, m); if (i < 9) rdB((i + 1) & 1, i + 1, m); if (m & 1) jobA(m >> 1); SCHED(); }
      if (i < 9) {
        WAITL0();
        if (i < 6) waitv<10>();
        else if (i == 6) {
          asm volatile("s_waitcnt vmcnt(10)" : "+v"(efv[0]), "+v"(efv[1]) :: "memory");
          if (w == 1) {
            const unsigned tl = (unsigned)tag, th = (unsigned)(tag >> 32);
            const unsigned bad = (efv[0].x ^ tl) | (efv[0].y ^ th) | (efv[0].z ^ tl) | (efv[0].w ^ th) | (efv[1].x ^ tl) | (efv[1].y ^ th) | (efv[1].z ^ tl) | (efv[1].w ^ th);
            if (!WAVE_ALL(bad == 0u)) {
#pragma nounroll
              for (unsigned spins = 0; spins < SPIN_CAP; ++spins) {
                const v4u e0 = ldsc1(rs, FLAG_E_OFF + xcd * 2048 + lane * 32, 0), e1 = ldsc1(rs, FLAG_E_OFF + xcd * 2048 + lane * 32 + 16, 0);
                if (WAVE_ALL(((e0.x ^ tl) | (e0.y ^ th) | (e0.z ^ tl) | (e0.w ^ th) | (e1.x ^ tl) | (e1.y ^ th) | (e1.z ^ tl) | (e1.w ^ th)) == 0u)) break;
                asm volatile("s_sleep 2" ::: "memory");
              }
            }
            if (b == 0 && lane == 0) __hip_atomic_store((GAS u64*)(ws + EPOCH_OFF), tag, RLX_AGENT);
          }
        }
        else if (i == 7) waitv<13>();
        else waitv<24>();
        BAR();
      }
      if (i == 2 && tid < 8) __hip_atomic_store((GAS u64*)(ws + FLAG_E_OFF) + tid * 256 + b, tag, RLX_AGENT);
      if (i == 5) {
        asm volatile("s_cmp_eq_u32 %4, 1\n\ts_cselect_b64 exec, -1, 0\n\tglobal_load_dwordx4 %0, %2, %3 sc1\n\tglobal_load_dwordx4 %1, %2, %3 offset:16 sc1\n\ts_mov_b64 exec, -1"
                     : "=&v"(efv[0]), "=&v"(efv[1]) : "v"(l16 * 2), "s"(ws + FLAG_E_OFF + xcd * 2048), "s"(w) : "memory", "scc");
      }
      if (i == 8 && w == 3) {
        const int l48 = lane < 48 ? lane : 47;
        f32x2 a = *(LAS f32x2*)(sm + ESR + l48 * 8);
#pragma unroll
        for (int k = 1; k < 16; ++k) a += *(LAS f32x2*)(sm + ESR + k * 384 + l48 * 8);
        *(LAS f32x2*)(sm + CSF + l48 * 8) = a;
        lds_signal(cnt + 8, lane);
      }
      SCHED();
#pragma unroll
      for (int m = 14; m < 20; ++m) { mmB(i & 1, m); jobB(m - 14); SCHED(); }
    };
    stepB(IC(1)); stepB(IC(2)); stepB(IC(3)); stepB(IC(4)); stepB(IC(5)); stepB(IC(6)); stepB(IC(7)); stepB(IC(8)); stepB(IC(9));
    __builtin_amdgcn_s_setprio(2);
    f32x2 ur[8], ui[8], er, ei;
    float u4[8], e4;
    ls4 = (c & 1) ? ls4 : -ls4;
    {
      const float rsa[4] = {st0[0], st0[2], st1[0], st1[2]}, rsb[4] = {st2_[0], st2_[2], st3[0], st3[2]};
      f32x2 mba[4] = {{st0[1], st0[1]}, {st0[3], st0[3]}, {st1[1], st1[1]}, {st1[3], st1[3]}}, mbb[4] = {{st2_[1], st2_[1]}, {st2_[3], st2_[3]}, {st3[1], st3[1]}, {st3[3], st3[3]}};
#pragma unroll
      for (int s = 0; s < 4; ++s) { asm volatile("" : "+v"(mba[s])); asm volatile("" : "+v"(mbb[s])); }
      const f32x2 Sr = {Sc[0], Sc[2]}, Si = {Sc[1], Sc[3]};
#pragma unroll
      for (int s = 0; s < 4; ++s) {
        ur[s] = (f32x2{acc[0][0][s], acc[0][2][s]} - mba[s] * Sr) * rsa[s]; ui[s] = (f32x2{acc[0][1][s], acc[0][3][s]} - mba[s] * Si) * rsa[s];
        ur[4 + s] = (f32x2{acc[1][0][s], acc[1][2][s]} - mbb[s] * Sr) * rsb[s]; ui[4 + s] = (f32x2{acc[1][1][s], acc[1][3][s]} - mbb[s] * Si) * rsb[s];
        u4[s] = (acc[0][4][s] - mba[s][0] * Sc[4]) * rsa[s]; u4[4 + s] = (acc[1][4][s] - mbb[s][0] * Sc[4]) * rsb[s];
      }
      er = ur[0]; ei = ui[0]; e4 = u4[0];
#pragma unroll
      for (int s = 1; s < 8; ++s) {
        const f32x2 nr = lr2 * er + (ur[s] - li2 * ei), ni = lr2 * ei + (li2 * er + ui[s]);
        er = nr; ei = ni;
        e4 = lr4 * e4 + (ls4 * swp1(e4) + u4[s]);
      }
    }
    lds_wait(cnt + 8, 1u);
    LAS float* es = (LAS float*)(sm + ESR);
    es[(4 * w + q) * 80 + 2 * c] = er[0]; es[(4 * w + q) * 80 + 2 * c + 1] = ei[0];
    es[(4 * w + q) * 80 + 32 + 2 * c] = er[1]; es[(4 * w + q) * 80 + 33 + 2 * c] = ei[1];
    es[(4 * w + q) * 80 + 64 + c] = e4;
    lds_signal(cnt + 0, lane);
    const int l40 = lane < 40 ? lane : 39;
    const unsigned tlo = (unsigned)tag >> 24 << 24, thi = (unsigned)(tag >> 32);
    if (w == 0) {
      const float l8r = pm[80 + l40], l8i = pm[120 + l40];
      lds_wait(cnt + 0, 4u);
      f32x2 ev[16];
#pragma unroll
      for (int s = 0; s < 16; ++s) ev[s] = *(LAS f32x2*)(es + s * 80 + 2 * l40);
      float Er = 0.f, Ei = 0.f;
#pragma unroll
      for (int s = 0; s < 16; ++s) {
        const float pr_ = Er, pi_ = Ei;
        Er = __builtin_fmaf(l8r, pr_, __builtin_fmaf(-l8i, pi_, ev[s][0])); Ei = __builtin_fmaf(l8r, pi_, __builtin_fmaf(l8i, pr_, ev[s][1]));
        asm volatile("" : "+v"(Er)); asm volatile("" : "+v"(Ei));
        ev[s] = f32x2{pr_, pi_};
      }
      if (r < 15 && lane < 40) {
        v4u o; o.x = tlo | ((__builtin_bit_cast(unsigned, Er) + 0x80u) >> 8); o.y = thi; o.z = tlo | ((__builtin_bit_cast(unsigned, Ei) + 0x80u) >> 8); o.w = thi;
        __builtin_amdgcn_raw_buffer_store_b128(o, rs, EG_OFF + ((j * 16 + r) * 40 + lane) * 16, 0, AUX_SC1);
      }
#pragma unroll
      for (int s = 0; s < 16; ++s) *(LAS f32x2*)(es + s * 80 + 2 * l40) = ev[s];
    }
    waitv<15>();
    if (lane == 0) __hip_atomic_fetch_add(cnt + 4 + w, 1u, RLX_WG);
#pragma unroll
    for (int k = PBY; k < DBY; ++k)
#pragma unroll
      for (int n = 0; n < 5; ++n) bw[k][n] = ldsc1(rs, boff + n * 1024, rotk<10>(k, s2) * 5120);
    SCHED();
    f32x2 gr = {1.f, 1.f}, gi = {0.f, 0.f}, g4r = {1.f, 1.f}, g4i = {0.f, 0.f};
    { f32x2 pr = {pm[80 + c], pm[96 + c]}, pi = {pm[120 + c], pm[136 + c]};
      f32x2 qr = {pm[112 + (c >> 1)], 0.f}, qi = {pm[152 + (c >> 1)], 0.f};
      const int sg = 4 * w + q;
#pragma unroll
      for (int bt = 0; bt < 4; ++bt) {
        const f32x2 tr_ = gr * pr - gi * pi, ti_ = gr * pi + gi * pr, ur_ = g4r * qr - g4i * qi, ui_ = g4r * qi + g4i * qr;
        if ((sg >> bt) & 1) { gr = tr_; gi = ti_; g4r = ur_; g4i = ui_; }
        if (bt < 3) { const f32x2 n1 = pr * pr - pi * pi, n2 = qr * qr - qi * qi; pi = 2.f * pr * pi; pr = n1; qi = 2.f * qr * qi; qr = n2; }
      } }
    const float g4s = (c & 1) ? g4i[0] : -g4i[0];
    const f32x2 cr2 = f32x2{2.f * pm[240 + c], 2.f * pm[256 + c]}, ci2 = f32x2{2.f * pm[280 + c], 2.f * pm[296 + c]};
    const float cr4 = pm[272 + (c >> 1)], ca4 = (c & 1) ? -2.f * cr4 : 2.f * cr4, cb4 = -2.f * pm[312 + (c >> 1)];
    int hoffs[5];
#pragma unroll
    for (int m = 0; m < 5; ++m) {
      const int unit = 4 * m + g, kap = 10 * j + (unit >> 1);
      hoffs[m] = HT_OFF + (((2 * r + (w >> 1)) * 40 + (kap >> 2)) * 4 + 2 * (w & 1) + (unit & 1)) * 1024 + (kap & 3) * 256 + n16 * 16;
      asm volatile("" : "+v"(hoffs[m]));
    }
    if (w < 2) {
      float Hr_ = 0.f, Hi_ = 0.f;
      if (r > 8 * w) {
        const float lKr = pm[160 + l40], lKi = pm[200 + l40];
        float p4r, p4i, p8r, p8i;
        { const float a2r = lKr * lKr - lKi * lKi, a2i = 2.f * lKr * lKi; p4r = a2r * a2r - a2i * a2i; p4i = 2.f * a2r * a2i; p8r = p4r * p4r - p4i * p4i; p8i = 2.f * p4r * p4i; }
        const int eo = EG_OFF + (j * 16 * 40 + l40) * 16, top = r - 1 - 8 * w;
        v4u gv[8];
#pragma nounroll
        for (unsigned spins = 0; spins < SPIN_CAP; ++spins) {
#pragma unroll
          for (int m = 0; m < 8; ++m) gv[m] = ldsc1(rs, eo, (m <= top ? top - m : 0) * 640);
          unsigned bad = 0u, bad2 = 0u;
#pragma unroll
          for (int m = 0; m < 8; ++m) { bad |= (gv[m].x ^ tlo) | (gv[m].z ^ tlo); bad2 |= (gv[m].y ^ thi) | (gv[m].w ^ thi); }
          if (WAVE_ALL(((bad >> 24) | bad2) == 0u)) break;
          asm volatile("s_sleep 1" ::: "memory");
        }
        float hr_[2] = {0.f, 0.f}, hi_[2] = {0.f, 0.f};
#pragma unroll
        for (int m = 3; m >= 0; --m)
#pragma unroll
          for (int k = 0; k < 2; ++k) {
            const bool ok = 4 * k + m <= top;
            const float er_ = ok ? __builtin_bit_cast(float, gv[4 * k + m].x << 8) : 0.f, ei_ = ok ? __builtin_bit_cast(float, gv[4 * k + m].z << 8) : 0.f;
            const float nr_ = __builtin_fmaf(lKr, hr_[k], __builtin_fmaf(-lKi, hi_[k], er_)), ni_ = __builtin_fmaf(lKr, hi_[k], __builtin_fmaf(lKi, hr_[k], ei_));
            hr_[k] = nr_; hi_[k] = ni_;
            asm volatile("" : "+v"(hr_[k])); asm volatile("" : "+v"(hi_[k]));
          }
        Hr_ = p4r * hr_[1] - p4i * hi_[1] + hr_[0]; Hi_ = p4r * hi_[1] + p4i * hr_[1] + hi_[0];
        asm volatile("" : "+v"(Hr_)); asm volatile("" : "+v"(Hi_));
        if (w == 1) { const float t_ = p8r * Hr_ - p8i * Hi_; Hi_ = p8r * Hi_ + p8i * Hr_; Hr_ = t_; asm volatile("" : "+v"(Hr_)); asm volatile("" : "+v"(Hi_)); }
      }
      ((LAS float*)(sm + HIN))[80 * w + 2 * l40] = Hr_; ((LAS float*)(sm + HIN))[80 * w + 2 * l40 + 1] = Hi_;
      lds_signal(cnt + 1, lane);
    }
    {
      lds_wait(cnt + 1, 2u);
      LAS unsigned char* scr = sm + w * 10240;
      LAS float* hin = (LAS float*)(sm + HIN);
      const f32x2 Hr = {hin[2 * c] + hin[80 + 2 * c], hin[32 + 2 * c] + hin[112 + 2 * c]}, Hi = {hin[2 * c + 1] + hin[81 + 2 * c], hin[33 + 2 * c] + hin[113 + 2 * c]};
      f32x2 hr = {es[(4 * w + q) * 80 + 2 * c], es[(4 * w + q) * 80 + 32 + 2 * c]}, hi = {es[(4 * w + q) * 80 + 2 * c + 1], es[(4 * w + q) * 80 + 33 + 2 * c]};
      hr += gr * Hr - gi * Hi; hi += gr * Hi + gi * Hr;
      float h4 = es[(4 * w + q) * 80 + 64 + c] + (g4r[0] * (hin[64 + c] + hin[144 + c]) + g4s * (hin[64 + (c ^ 1)] + hin[144 + (c ^ 1)])), hp4 = swp1(h4);
      f32x2 o2[2][8]; float o4[8];
#pragma unroll
      for (int s = 0; s < 8; ++s) {
        const f32x2 nr = lr2 * hr + (ur[s] - li2 * hi), ni = lr2 * hi + (li2 * hr + ui[s]);
        hr = nr; hi = ni;
        o2[0][s] = cr2 * hr - ci2 * hi;
        o2[1][s] = -(cr2 * hi + ci2 * hr);
        h4 = lr4 * h4 + (ls4 * hp4 + u4[s]); hp4 = swp1(h4);
        o4[s] = ca4 * h4 + cb4 * hp4;
      }
#pragma unroll
      for (int p = 0; p < 2; ++p)
#pragma unroll
        for (int pt = 0; pt < 2; ++pt) {
          v4u ov; ov.x = pk2(o2[pt][0][p], o2[pt][1][p]); ov.y = pk2(o2[pt][2][p], o2[pt][3][p]); ov.z = pk2(o2[pt][4][p], o2[pt][5][p]); ov.w = pk2(o2[pt][6][p], o2[pt][7][p]);
          *(LAS v4u*)(scr + (32 * p + 2 * c + pt) * 80 + q * 16) = ov;
        }
      { v4u ov; ov.x = pk2(o4[0], o4[1]); ov.y = pk2(o4[2], o4[3]); ov.z = pk2(o4[4], o4[5]); ov.w = pk2(o4[6], o4[7]);
        *(LAS v4u*)(scr + (64 + c) * 80 + q * 16) = ov; }
      WAITL0();
      LAS unsigned char* ra = scr + (8 * (g >> 1) + ((lane & 15) >> 2)) * 80 + (16 * (g & 1) + 4 * (lane & 3)) * 2;
      v2u lo[5], hi_[5];
#pragma unroll
      for (int m = 0; m < 5; ++m) { lo[m] = trread(ra + m * 1280); hi_[m] = trread(ra + m * 1280 + 320); }
#pragma unroll
      for (int m = 0; m < 5; ++m) {
        v4u ov; ov.x = lo[m].x; ov.y = lo[m].y; ov.z = hi_[m].x; ov.w = hi_[m].y;
        __builtin_amdgcn_raw_buffer_store_b128(ov, rs, hoffs[m], 0, AUX_SC1);
      }
    }
    SCHED();
    waitv<0>();
    if (lane == 0) __hip_atomic_store((GAS u64*)(ws + FLAG_H_OFF) + ((r * 2 + (w >> 1)) * 16 + j) * 2 + (w & 1), tag, RLX_AGENT);
    __builtin_amdgcn_s_setprio(0);
    poll_flags((const GAS u64*)(ws + FLAG_H_OFF) + ((r * 2 + half2) * 16 + 4 * w) * 2 + (lane & 7), tag);
    f32x4 ac2[5][4];
#pragma unroll
    for (int n = 0; n < 5; ++n)
#pragma unroll
      for (int i = 0; i < 4; ++i) ac2[n][i] = f32x4{0.f, 0.f, 0.f, 0.f};
    int aoff[4];
#pragma unroll
    for (int i = 0; i < 4; ++i) aoff[i] = HT_OFF + ((rt64 * 40 + 10 * w) * 4 + ((i + w) & 3)) * 1024 + lane * 16;
    const size_t orow = (size_t)(128 * r + 64 * half2 + 16 * w + n16) * D + 80 * ct + 4 * g;
    v2u xv[5];
    stream_gemm<10, DBY, DAY, NLY>(rs, boff, aoff, s2, bw, ac2,
        [&](int k) __attribute__((always_inline)) { return sm + yslot(w, k) + lane * 16; },
        [&]() __attribute__((always_inline)) {
#pragma unroll
          for (int n = 0; n < 5; ++n) xv[n] = *(const v2u*)(x + orow + 16 * n);
        });
    auto ypart = [&](int ww, int i) __attribute__((always_inline)) { return sm + (i < 2 ? ww * 10240 + i * 5120 : yslot(ww, 0)) + lane * 16; };
#pragma unroll
    for (int i = 1; i < 4; ++i)
#pragma unroll
      for (int n = 0; n < 5; ++n) *(LAS f32x4*)(ypart(w, i - 1) + n * 1024) = ac2[n][i];
    lds_signal(cnt + 3, lane);
    lds_wait(cnt + 10 + w, 1u);
    v4u gs[5];
#pragma unroll
    for (int n = 0; n < 5; ++n) {
      const v2u gq = *(LAS v2u*)(sm + fslot(w, 0) + n * 512 + lane * 8), sq = *(LAS v2u*)(sm + SKR + w * 2560 + n * 512 + lane * 8);
      gs[n] = v4u{gq.x, gq.y, sq.x, sq.y};
    }
    lds_wait(cnt + 3, 4u);
#pragma unroll
    for (int n = 0; n < 5; ++n) {
      f32x4 y = ac2[n][0] + f32x4{bf2f(gs[n].z & 0xffffu), bfhi(gs[n].z), bf2f(gs[n].w & 0xffffu), bfhi(gs[n].w)};
#pragma unroll
      for (int o = 1; o < 4; ++o) y += *(LAS f32x4*)(ypart((w + o) & 3, 3 - o) + n * 1024);
      const f32x4 gg = {bf2f(gs[n].x & 0xffffu), bfhi(gs[n].x), bf2f(gs[n].y & 0xffffu), bfhi(gs[n].y)};
      const f32x4 xx = {bf2f(xv[n].x & 0xffffu), bfhi(xv[n].x), bf2f(xv[n].y & 0xffffu), bfhi(xv[n].y)};
      __builtin_nontemporal_store(xx + y * gg, (f32x4*)(out + orow + 16 * n));
    }
  } else {
    {
      const int line = (idx * 4 + w) * 3 + (lane < 3 ? lane : 2);
      if (line < CODE_LINES)
        asm volatile("s_mov_b32 m0, %2\n\ts_nop 0\n\tglobal_load_lds_dword %1, %0" :: "s"(pc0), "v"(line * 128), "s"(smb + JUNK) : "memory");
    }
    if (w == 0) {
      int o;
      if (lane < 8) o = FLAG_E_OFF + lane * 2048 + b * 8;
      else if (lane < 12) o = FLAG_F_OFF + (r * 2 + ((lane >> 1) & 1)) * 256 + (lane & 1) * 128;
      else if (lane < 16) o = FLAG_H_OFF + (r * 2 + ((lane >> 1) & 1)) * 256 + (lane & 1) * 128;
      else if (lane < 21) o = EG_OFF + (j * 16 + r) * 640 + (lane - 16) * 128;
      else if (lane < 24) o = PCF_OFF + (j * 16 + r) * 384 + (lane - 21) * 128;
      else if (lane < 27) o = PCB_OFF + j * 1024 + (lane - 24) * 128;
      else o = PRS_OFF + r * 1024 + j * 64;
      asm volatile("s_mov_b32 m0, %2\n\ts_nop 0\n\tglobal_load_lds_dword %1, %0 sc1" :: "s"(ws), "v"(o), "s"(smb + JUNK) : "memory");
    }
    const int t32 = lane & 31, hh = lane >> 5;
    const int fx = (((t32 >> 3) & 3) << 1) | ((t32 >> 1) & 1);
    const int xa0 = XS + (32 * w + t32) * 128 + ((hh ^ fx) << 4);
    const int wa0 = W1S + (8 * hh + ((lane & 15) >> 2)) * 192 + (16 * ((lane >> 4) & 1) + 4 * (lane & 3)) * 2;
    f32x16 acc[3];
#pragma unroll
    for (int n = 0; n < 3; ++n)
#pragma unroll
      for (int e = 0; e < 16; ++e) acc[n][e] = 0.f;
    v4u xf[2][4]; v2u wf[2][4][3][2];
    auto rdF = [&](int set, int st, int q) __attribute__((always_inline)) {
      LAS unsigned char* sb = sm + (st % 3) * STAGE;
      const int s = q / 7, e = q % 7;
      if (e == 0) xf[set][s] = *(LAS v4u*)(sb + (xa0 ^ (s * 32)));
      else wf[set][s][(e - 1) >> 1][(e - 1) & 1] = trread(sb + wa0 + s * 3072 + ((e - 1) & 1) * 768 + ((e - 1) >> 1) * 64);
    };
    auto wfrag = [&](int set, int s, int n) __attribute__((always_inline)) { v4u v; v.x = wf[set][s][n][0].x; v.y = wf[set][s][n][0].y; v.z = wf[set][s][n][1].x; v.w = wf[set][s][n][1].y; return v; };
    auto mmF = [&](int set, int m) __attribute__((always_inline)) { const int s = m / 3, n = m % 3; acc[n] = mfma32(wfrag(set, s, n), xf[set][s], acc[n]); };
    BAR();
#pragma unroll
    for (int q = 0; q < 28; ++q) rdF(0, 0, q);
    SCHED();
#pragma unroll
    for (int m = 0; m < 12; ++m) { mmF(0, m); SCHED(); }
    BAR();
    const unsigned ones = 0x3f803f80u;
    {
      {
        LAS unsigned char* xp = sm + XROW + (2 * w + hh) * 1280;
        const v4u xa = *(LAS v4u*)(xp + t32 * 16), xb = *(LAS v4u*)(xp + 512 + t32 * 16); const v2u xc = *(LAS v2u*)(xp + 1024 + t32 * 8);
        const unsigned xw[10] = {xa.x, xa.y, xa.z, xa.w, xb.x, xb.y, xb.z, xb.w, xc.x, xc.y};
        f32x2 xv2[10], a2 = {0.f, 0.f};
#pragma unroll
        for (int k = 0; k < 10; ++k) { xv2[k] = f32x2{bf2f(xw[k] & 0xffffu), bfhi(xw[k])}; a2 += xv2[k]; }
        const float mean = sum32(a2[0] + a2[1]) * (1.f / D);
        f32x2 q2 = {0.f, 0.f};
#pragma unroll
        for (int k = 0; k < 10; ++k) { const f32x2 d0 = xv2[k] - mean; q2 += d0 * d0; }
        const float var = sum32(q2[0] + q2[1]) * (1.f / D);
        const f32x2 st = {__builtin_amdgcn_rcpf(rbf(__builtin_amdgcn_sqrtf(rbf(rbf(var) + 1e-5f)))), rbf(mean)};
        if (t32 == 0) __builtin_amdgcn_raw_buffer_store_b64(__builtin_bit_cast(v2u, st), rs, PRS_OFF + (r * 128 + 8 * j + 2 * w + hh) * 8, 0, AUX_SC1);
        SCHED();
      }
      {
        if (w < 3) {
          const int br = 2 * w + hh > 4 ? 4 : 2 * w + hh;
          LAS unsigned char* xp = sm + BROW + br * 1280;
          const v4u xa = *(LAS v4u*)(xp + t32 * 16), xb = *(LAS v4u*)(xp + 512 + t32 * 16); const v2u xc = *(LAS v2u*)(xp + 1024 + t32 * 8);
          const int gl = (lane >> 4) & 1, f16 = 3 * fu + w;
          v2u lo[3], hi[3];
#pragma unroll
          for (int q3 = 0; q3 < 3; ++q3) {
            const int nt = q3 < 2 ? 2 * q3 + hh : 4;
            LAS unsigned char* ra = sm + W2SCR + (16 * w + 4 * gl + ((lane & 15) >> 2)) * 160 + (16 * nt + 4 * (lane & 3)) * 2;
            lo[q3] = trread(ra); hi[q3] = trread(ra + 8 * 160);
          }
          const float c1 = sum32(dot2(xc.y, ones, dot2(xc.x, ones, sum8(xb, sum8(xa, 0.f)))));
          const int chn = 5 * (r & 7) + br, part = r >> 3;
          const int col = chn < 32 ? 32 * (chn >> 4) + 16 * part + (chn & 15) : 2 * chn + part;
          if (t32 == 0) __builtin_amdgcn_raw_buffer_store_b32(__builtin_bit_cast(unsigned, c1), rs, PCB_OFF + (j * 256 + col) * 4, 0, AUX_SC1);
#pragma unroll
          for (int q3 = 0; q3 < 3; ++q3) {
            const int nt = q3 < 2 ? 2 * q3 + hh : 4;
            v4u ov; ov.x = lo[q3].x; ov.y = lo[q3].y; ov.z = hi[q3].x; ov.w = hi[q3].y;
            __builtin_amdgcn_raw_buffer_store_b128(ov, rs, WT_OFF + ((ctu * 88 + 40 + (f16 >> 1)) * 5 + nt) * 1024 + (2 * (f16 & 1) + gl) * 256 + n16 * 16, 0, AUX_SC1);
          }
        } else {
          float cs6[6] = {0.f, 0.f, 0.f, 0.f, 0.f, 0.f};
#pragma unroll
          for (int m = 0; m < 3; ++m) {
            const int kq = m < 2 ? g + 4 * m : 8 + (g & 1);
            const unsigned wgt = (m < 2 || g < 2) ? ones : 0u;
            LAS unsigned char* ra = sm + W1SCR + (4 * kq + ((lane & 15) >> 2)) * 192 + 8 * (lane & 3);
#pragma unroll
            for (int c6 = 0; c6 < 6; ++c6) { const v2u v = trread(ra + c6 * 32); cs6[c6] = dot2(v.y, wgt, dot2(v.x, wgt, cs6[c6])); }
          }
#pragma unroll
          for (int c6 = 0; c6 < 6; ++c6) cs6[c6] = add32(add16(cs6[c6]));
          const float v0 = g == 0 ? cs6[0] : g == 1 ? cs6[1] : g == 2 ? cs6[2] : cs6[3], v1 = (g & 1) ? cs6[5] : cs6[4];
          const int po = PCF_OFF + (j * 16 + r) * 384;
          __builtin_amdgcn_raw_buffer_store_b32(__builtin_bit_cast(unsigned, v0), rs, po + lane * 4, 0, AUX_SC1);
          __builtin_amdgcn_raw_buffer_store_b32(__builtin_bit_cast(unsigned, v1), rs, po + 256 + t32 * 4, 0, AUX_SC1);
        }
        SCHED();
      }
      {
        LAS u16* raw = (LAS u16*)(sm + RAW);
        if (w == 0) {
          const int l40 = lane < 40 ? lane : 39;
          const float dt = rbf(__expf(bf2f(raw[80 + l40])));
          const float lr = bf2f(raw[l40]), li = bf2f(raw[40 + l40]);
          const float mag = __expf(dt * lr);
          float rev = dt * li * 0.15915494309189535f; rev -= floorf(rev);
          const float ar = mag * __builtin_amdgcn_cosf(rev), ai = mag * __builtin_amdgcn_sinf(rev);
          const float nr = ar - 1.f, den = __builtin_amdgcn_rcpf(lr * lr + li * li);
          const float cr = (nr * lr + ai * li) * den, ci = (ai * lr - nr * li) * den;
          float pr = ar, pi = ai;
#pragma unroll
          for (int s = 0; s < 3; ++s) { const float t = pr * pr - pi * pi; pi = 2.f * pr * pi; pr = t; }
          const float p8r = pr, p8i = pi;
#pragma unroll
          for (int s = 0; s < 4; ++s) { const float t = pr * pr - pi * pi; pi = 2.f * pr * pi; pr = t; }
          LAS float* pm = (LAS float*)(sm + PRM) + l40;
          pm[0] = ar; pm[40] = ai; pm[80] = p8r; pm[120] = p8i; pm[160] = pr; pm[200] = pi; pm[240] = cr; pm[280] = ci;
        } else if (w == 1) {
          const int l48 = lane < 48 ? lane : 47;
          const unsigned v = *(LAS unsigned*)(sm + RAW + 240 + l48 * 4);
          *(LAS f32x2*)(sm + B1F + l48 * 8) = f32x2{bf2f(v & 0xffffu), bfhi(v)};
        } else {
          const int l40 = lane < 40 ? lane : 39;
          const unsigned v = *(LAS unsigned*)(sm + RAW + 432 + (w - 2) * 160 + l40 * 4);
          *(LAS f32x2*)(sm + DB2 + (w - 2) * 320 + l40 * 8) = f32x2{bf2f(v & 0xffffu), bfhi(v)};
        }
        SCHED();
      }
    }
    BAR();
#pragma unroll
    for (int q = 0; q < 28; ++q) rdF(1, 1, q);
    WAITL0(); BAR();
    const u64 tag = read_tag();
    v4u bw[DBF][5];
    const int boff = WT_OFF + (ct * 88 + 40 + 12 * w) * 5120 + lane * 16;
    const int s2 = s2f;
    auto stepF = [&](auto I_) __attribute__((always_inline)) {
      constexpr int i = decltype(I_)::value;
#pragma unroll
      for (int m = 0; m < 8; ++m) {
        mmF(i & 1, m);
        if (i < 9) {
#pragma unroll
          for (int q = (m < 4 ? 4 * m : 3 * m + 4); q < (m < 4 ? 4 * m + 4 : 3 * m + 7); ++q) rdF((i + 1) & 1, i + 1, q);
        }
        SCHED();
      }
      if (i < 9) {
        WAITL0();
        if (i == 2) waitv<0>();
        BAR();
      }
      SCHED();
#pragma unroll
      for (int m = 8; m < 12; ++m) {
        mmF(i & 1, m);
        if constexpr (i >= 8) {
          bw[i - 8][m - 8] = ldsc1(rs, boff + (m - 8) * 1024, rotk<12>(i - 8, s2) * 5120);
          if (m == 11) bw[i - 8][4] = ldsc1(rs, boff + 4096, rotk<12>(i - 8, s2) * 5120);
        }
        SCHED();
      }
    };
    stepF(IC(1)); stepF(IC(2)); stepF(IC(3)); stepF(IC(4)); stepF(IC(5)); stepF(IC(6)); stepF(IC(7)); stepF(IC(8)); stepF(IC(9));
    {
      lds_wait(cnt + 8, 1u);
      const f32x2 st2 = *(LAS f32x2*)(sm + ST2 + (32 * w + t32) * 8);
      f32x4 S4[12], b4[12];
#pragma unroll
      for (int k = 0; k < 12; ++k) { S4[k] = *(LAS f32x4*)(sm + CSF + (8 * k + 4 * hh) * 4); b4[k] = *(LAS f32x4*)(sm + B1F + (8 * k + 4 * hh) * 4); }
      const float rsd = st2[0], nm = -rsd * st2[1];
      const int hoff = H1T_OFF + (((2 * r + (w >> 1)) * 48 + 3 * j) * 4 + 2 * (w & 1) + (t32 >> 4)) * 1024 + hh * 256 + (t32 & 15) * 16;
#pragma unroll
      for (int n = 0; n < 3; ++n) {
        unsigned ou[8];
#pragma unroll
        for (int e = 0; e < 8; ++e) {
          const f32x2 a = {acc[n][2 * e], acc[n][2 * e + 1]};
          const f32x4 S = S4[4 * n + (e >> 1)], bb = b4[4 * n + (e >> 1)];
          const f32x2 S2 = {S[2 * (e & 1)], S[2 * (e & 1) + 1]}, b2_ = {bb[2 * (e & 1)], bb[2 * (e & 1) + 1]};
          const f32x2 v = rsd * a + (nm * S2 + b2_);
          const f32x2 z = v * (-2.302208198f - 0.1029432397f * (v * v));
          f32x2 d = {__builtin_amdgcn_exp2f(z[0]), __builtin_amdgcn_exp2f(z[1])};
          d += 1.f;
          const f32x2 rc = {__builtin_amdgcn_rcpf(d[0]), __builtin_amdgcn_rcpf(d[1])};
          const f32x2 gl = v * rc;
          ou[e] = pk2(gl[0], gl[1]);
        }
        __builtin_amdgcn_raw_buffer_store_b128(v4u{ou[0], ou[1], ou[2], ou[3]}, rs, hoff + n * 4096, 0, AUX_SC1);
        __builtin_amdgcn_raw_buffer_store_b128(v4u{ou[4], ou[5], ou[6], ou[7]}, rs, hoff + n * 4096 + 512, 0, AUX_SC1);
        SCHED();
      }
    }
    v2u xv[5];
    { const u16* xp = x + (size_t)(128 * r + 64 * half2 + 16 * w + n16) * D + 80 * ct + 4 * g;
#pragma unroll
      for (int n = 0; n < 5; ++n) xv[n] = *(const v2u*)(xp + 16 * n); }
    SCHED();
    waitv<5>();
    if (lane == 0) __hip_atomic_store((GAS u64*)(ws + FLAG_F_OFF) + ((r * 2 + (w >> 1)) * 16 + j) * 2 + (w & 1), tag, RLX_AGENT);
#pragma unroll
    for (int k = PBF; k < DBF; ++k)
#pragma unroll
      for (int n = 0; n < 5; ++n) bw[k][n] = ldsc1(rs, boff + n * 1024, rotk<12>(k, s2) * 5120);
    const int trow = 64 * half2 + 16 * w + n16;
    {
      float est[2] = {((LAS float*)(sm + ST2))[2 * trow], ((LAS float*)(sm + ST2))[2 * trow + 1]};
      asm volatile("" : "+v"(est[0])); asm volatile("" : "+v"(est[1]));
#pragma unroll
      for (int n = 0; n < 5; ++n) {
        const f32x4 ds = *(LAS f32x4*)(sm + DB2 + (16 * n + 4 * g) * 4);
        const unsigned d0 = pk2(bf2f(xv[n].x & 0xffffu) - est[1], bfhi(xv[n].x) - est[1]), d1 = pk2(bf2f(xv[n].y & 0xffffu) - est[1], bfhi(xv[n].y) - est[1]);
        const unsigned n0 = pk2(bf2f(d0 & 0xffffu) * est[0], bfhi(d0) * est[0]), n1 = pk2(bf2f(d1 & 0xffffu) * est[0], bfhi(d1) * est[0]);
        *(LAS v2u*)(sm + SKR + w * 2560 + n * 512 + lane * 8) = v2u{pk2(bf2f(n0 & 0xffffu) * ds[0], bfhi(n0) * ds[1]), pk2(bf2f(n1 & 0xffffu) * ds[2], bfhi(n1) * ds[3])};
      }
    }
    poll_flags((const GAS u64*)(ws + FLAG_F_OFF) + ((r * 2 + half2) * 16 + 4 * w) * 2 + (lane & 7), tag);
    f32x4 ac2[5][4];
#pragma unroll
    for (int n = 0; n < 5; ++n)
#pragma unroll
      for (int i = 0; i < 4; ++i) ac2[n][i] = f32x4{0.f, 0.f, 0.f, 0.f};
    int aoff[4];
#pragma unroll
    for (int i = 0; i < 4; ++i) aoff[i] = H1T_OFF + ((rt64 * 48 + 12 * w) * 4 + ((i + w) & 3)) * 1024 + lane * 16;
    __builtin_amdgcn_s_setprio(1);
    stream_gemm<12, DBF, DAF, NLF>(rs, boff, aoff, s2, bw, ac2,
        [&](int k) __attribute__((always_inline)) { if (k == 0) lds_wait(cnt + 4 + w, 1u); return sm + fslot(w, k) + lane * 16; },
        [&]() __attribute__((always_inline)) {});
#pragma unroll
    for (int i = 1; i < 4; ++i)
#pragma unroll
      for (int n = 0; n < 5; ++n) *(LAS f32x4*)(sm + fslot(w, i - 1) + n * 1024 + lane * 16) = ac2[n][i];
    lds_signal(cnt + 2, lane);
    v2u gs[5];
    lds_wait(cnt + 2, 4u);
    f32x4 h2[5];
#pragma unroll
    for (int n = 0; n < 5; ++n) {
      h2[n] = ac2[n][0];
#pragma unroll
      for (int o = 1; o < 4; ++o) h2[n] += *(LAS f32x4*)(sm + fslot((w + o) & 3, 3 - o) + n * 1024 + lane * 16);
    }
    lds_signal(cnt + 9, lane);
#pragma unroll
    for (int n = 0; n < 5; ++n) {
      const f32x4 bs = *(LAS f32x4*)(sm + DB2 + 320 + (16 * n + 4 * g) * 4);
      const unsigned r0 = pk2(h2[n][0], h2[n][1]), r1 = pk2(h2[n][2], h2[n][3]);
      const unsigned a0 = pk2(bf2f(r0 & 0xffffu) + bs[0], bfhi(r0) + bs[1]), a1 = pk2(bf2f(r1 & 0xffffu) + bs[2], bfhi(r1) + bs[3]);
      const float a[4] = {bf2f(a0 & 0xffffu), bfhi(a0), bf2f(a1 & 0xffffu), bfhi(a1)};
      float sg[4];
#pragma unroll
      for (int e = 0; e < 4; ++e) sg[e] = a[e] * __builtin_amdgcn_rcpf(1.f + __builtin_amdgcn_exp2f(-1.4426950408889634f * a[e]));
      gs[n].x = pk2(sg[0], sg[1]); gs[n].y = pk2(sg[2], sg[3]);
      asm volatile("" : "+v"(gs[n]));
    }
    lds_wait(cnt + 9, 4u);
#pragma unroll
    for (int n = 0; n < 5; ++n) *(LAS v2u*)(sm + fslot(w, 0) + n * 512 + lane * 8) = gs[n];
    lds_signal(cnt + 10 + w, lane);
  }
}
}

extern "C" void kernel_launch(void* const* d_in, const int* in_sizes, int n_in,
                              void* d_out, int out_size, void* d_ws, size_t ws_size,
                              hipStream_t stream) {
  using s5k::u16;
  (void)hipFuncSetAttribute((const void*)s5k::s5_fused, hipFuncAttributeMaxDynamicSharedMemorySize, s5k::LDS_BYTES);
  s5k::s5_fused<<<s5k::NWG, s5k::NTHR, s5k::LDS_BYTES, stream>>>(
      (const u16*)d_in[0], (const u16*)d_in[1], (const u16*)d_in[2], (const u16*)d_in[3], (const u16*)d_in[4], (const u16*)d_in[5],
      (const u16*)d_in[6], (const u16*)d_in[7], (const u16*)d_in[8], (const u16*)d_in[10], (const u16*)d_in[11], (const u16*)d_in[12],
      (const u16*)d_in[13], (float*)d_out, (unsigned char*)d_ws);
}
```
